```python
import torch

def _softmax(x, axis=-1):
    return torch.softmax(x, dim=axis)
def _gelu(x):
    return torch.nn.functional.gelu(x, approximate="tanh")
def _silu(x):
    return torch.nn.functional.silu(x)
def _sigmoid(x):
    return torch.sigmoid(x)
def _layernorm(x, axis=-1, eps=1e-5):
    m = x.mean(dim=axis, keepdim=True)
    v = x.var(dim=axis, keepdim=True, unbiased=False)
    return (x - m) / torch.sqrt(v + eps)
def _scatter_add(tgt, idx, src, axis=0):
    return tgt.clone().index_add_(axis, idx, src)
def _segment_sum(src, seg, n):
    out = torch.zeros((n,) + src.shape[1:], dtype=src.dtype)
    return out.index_add_(0, seg, src)
def _pad(x, pw):
    flat = tuple(v for pair in reversed(pw) for v in pair)
    return torch.nn.functional.pad(x, flat)

kernel_name = 'standalone__linear_kernel_rfa__prompt'

def setup_inputs():

    _w = (lambda a: (lambda t: t.to(torch.bfloat16) if t.is_floating_point() else t)(torch.as_tensor(a)))
    B, H, Sq, Sk, D, M = 1, 32, 2048, 2048, 128, 64
    return {
        "q": _w((lambda *s: torch.randn(*s, dtype=torch.bfloat16) * 0.05)(B, H, Sq, D)),
        "k": _w((lambda *s: torch.randn(*s, dtype=torch.bfloat16) * 0.8)(B, H, Sk, D)),
        "v": _w((lambda *s: torch.randn(*s, dtype=torch.bfloat16) * 0.8)(B, H, Sk, D)),
        "omega": _w((lambda *s: torch.randn(*s, dtype=torch.bfloat16) * 0.8)(M, D)),
    }

def reference(q, k, v, omega):
    d = q.shape[-1]
    m = omega.shape[0]
    scale = float(d) ** -0.25
    qs = q * scale
    ks = k * scale
    q_proj = torch.einsum("bhsd,md->bhsm", qs, omega)
    k_proj = torch.einsum("bhsd,md->bhsm", ks, omega)
    q_sq = 0.5 * torch.einsum("bhsd,bhsd->bhs", qs, qs)[..., None]
    k_sq = 0.5 * torch.einsum("bhsd,bhsd->bhs", ks, ks)[..., None]
    inv_sqrt_m = float(m) ** -0.5
    phi_q = torch.exp(q_proj - q_sq) * inv_sqrt_m + 1e-6
    phi_k = torch.exp(k_proj - k_sq) * inv_sqrt_m + 1e-6
    kv = torch.einsum("bhsm,bhsd->bhmd", phi_k, v)
    z  = torch.einsum("bhsm->bhm", phi_k)
    num = torch.einsum("bhqm,bhmd->bhqd", phi_q, kv)
    den = torch.einsum("bhqm,bhm->bhq", phi_q, z)[..., None] + 1e-6
    return num / den
```

```cpp
#include <hip/hip_runtime.h>
#include <hip/hip_bf16.h>
#include <stdint.h>

typedef __bf16 bf16x8 __attribute__((ext_vector_type(8)));
typedef __bf16 bf16x2 __attribute__((ext_vector_type(2)));
typedef short s16x4 __attribute__((ext_vector_type(4)));
typedef float f32x16 __attribute__((ext_vector_type(16)));
typedef float f32x4 __attribute__((ext_vector_type(4)));
typedef unsigned u32x4 __attribute__((ext_vector_type(4)));
typedef unsigned u32x2 __attribute__((ext_vector_type(2)));
typedef unsigned long long u64;
#define LAS __attribute__((address_space(3)))
#define DI __device__ __forceinline__

#define LD_POL ""
#define ST_POL ""
#define QWAIT 5

constexpr int H = 32, S = 2048, D = 128, M = 64;
constexpr int LDS_A = 0;
constexpr int LDS_B = 65536;
constexpr int LDS_C = 131072;
constexpr int LDS_Z = LDS_C + 16384;
constexpr size_t WS_CTL = 0;
constexpr size_t WS_Z = 32768;
constexpr size_t WS_KV = 32768 + 262144;
constexpr unsigned INVALID = 0xFFFFFFFFu;
constexpr unsigned SPIN_CAP = 1u << 20;

#define MFMA(a, b, c) __builtin_amdgcn_mfma_f32_32x32x16_bf16((a), (b), (c), 0, 0, 0)
#define WAIT_VM(n) asm volatile("s_waitcnt vmcnt(%0)" ::"n"(n) : "memory")
#define WAIT_LGKM0() asm volatile("s_waitcnt lgkmcnt(0)" ::: "memory")
#define BARRIER() do { asm volatile("s_waitcnt lgkmcnt(0)" ::: "memory"); __builtin_amdgcn_s_barrier(); asm volatile("" ::: "memory"); } while (0)

DI unsigned pk_bf16(float lo, float hi) { bf16x2 p; p[0] = (__bf16)lo; p[1] = (__bf16)hi; return __builtin_bit_cast(unsigned, p); }
DI bf16x8 as_frag(u32x4 v) { return __builtin_bit_cast(bf16x8, v); }
DI u32x4 lds_rd128(unsigned a) { return *(const LAS u32x4*)(size_t)a; }
DI void lds_wr128(unsigned a, u32x4 v) { *(LAS u32x4*)(size_t)a = v; }
DI void lds_wr64(unsigned a, u32x2 v) { *(LAS u32x2*)(size_t)a = v; }
DI float sumsq(u32x4 v, float acc) {
#pragma unroll
  for (int i = 0; i < 4; ++i) { const unsigned u = v[i]; const bf16x2 p = __builtin_bit_cast(bf16x2, u); acc = __builtin_amdgcn_fdot2_f32_bf16(p, p, acc, false); }
  return acc;
}
DI float add_halves(float x) {
  unsigned u = __builtin_bit_cast(unsigned, x);
  auto r = __builtin_amdgcn_permlane32_swap(u, u, false, false);
  return __builtin_bit_cast(float, (unsigned)r[0]) + __builtin_bit_cast(float, (unsigned)r[1]);
}

#define DMA_QUAD(lds, g, v0, v1, v2, v3)                                                        \
  asm volatile("s_mov_b32 m0, %0\n\ts_nop 0\n\t"                                                \
               "global_load_lds_dwordx4 %2, %1 offset:0 " LD_POL "\n\t"                         \
               "global_load_lds_dwordx4 %3, %1 offset:1024 " LD_POL "\n\t"                      \
               "global_load_lds_dwordx4 %4, %1 offset:2048 " LD_POL "\n\t"                      \
               "global_load_lds_dwordx4 %5, %1 offset:3072 " LD_POL                             \
               ::"s"(lds), "s"(g), "v"(v0), "v"(v1), "v"(v2), "v"(v3) : "memory")
#define DMA_TILE(lds, g, v0, v1, v2, v3)                                                        \
  do { DMA_QUAD(lds, g, v0, v1, v2, v3); DMA_QUAD((lds) + 4096, (g) + 4096, v0, v1, v2, v3); } while (0)
#define DMA_PAIR(lds, g, v0, v1, POL)                                                           \
  asm volatile("s_mov_b32 m0, %0\n\ts_nop 0\n\t"                                                \
               "global_load_lds_dwordx4 %2, %1 offset:0 " POL "\n\t"                            \
               "global_load_lds_dwordx4 %3, %1 offset:1024 " POL                                \
               ::"s"(lds), "s"(g), "v"(v0), "v"(v1) : "memory")
#define ST128_DEV(g, voff, val, off)                                                            \
  asm volatile("global_store_dwordx4 %0, %1, %2 offset:%3 sc1\n\ts_nop 1" ::"v"(voff), "v"(val), "s"(g), "n"(off) : "memory")
#define LD128_DEV(dst, g, voff, off)                                                            \
  asm volatile("global_load_dwordx4 %0, %1, %2 offset:%3 sc1" : "=v"(dst) : "v"(voff), "s"(g), "n"(off) : "memory")

DI bool words_valid(u32x4 a, u32x4 b) {
  unsigned m = max(max(max(a[0], a[1]), max(a[2], a[3])), max(max(b[0], b[1]), max(b[2], b[3])));
  return __builtin_amdgcn_ballot_w64(m == INVALID) == 0;
}

__global__ __launch_bounds__(512, 2) void rfa_kernel(const char* __restrict__ q, const char* __restrict__ k,
                                                     const char* __restrict__ v, const char* __restrict__ omega,
                                                     char* __restrict__ out, char* ws) {
  __shared__ __attribute__((aligned(16))) char smem[163840];
  const unsigned lds = (unsigned)(size_t)(LAS char*)smem;
  const int w = __builtin_amdgcn_readfirstlane(threadIdx.x >> 6);
  const unsigned lane = threadIdx.x & 63, r = lane & 31, h = lane >> 5;
  const int b = blockIdx.x, head = (b & 7) * 4 + (b >> 6), chunk = (b >> 3) & 7;
  const size_t row0 = (size_t)head * S + chunk * 256;
  const int mt = w >> 2, dt = w & 3;
  const bool zwave = (dt == mt);

  char* ctl = ws + WS_CTL + (size_t)(head * 8 + w) * 128;
  char* zbuf = ws + WS_Z + (size_t)(head * 8 + w) * 1024;
  char* kvbuf = ws + WS_KV + (size_t)(head * 8 + w) * 16384;
  const unsigned l16 = lane * 16;

  u32x2 ep;
  asm volatile("global_load_dwordx2 %0, %1, %2 offset:64 sc1" : "=v"(ep) : "v"(0u), "s"(ctl) : "memory");
  {
    const u32x4 inv = {INVALID, INVALID, INVALID, INVALID};
    char* mykv = kvbuf + chunk * 2048;
    ST128_DEV(mykv, l16, inv, 0);
    ST128_DEV(mykv, l16, inv, 1024);
    char* myz = zbuf + chunk * 128;
    if (lane < 8) ST128_DEV(myz, l16, inv, 0);
  }
  unsigned vsw[4];
#pragma unroll
  for (int i = 0; i < 4; ++i) vsw[i] = (lane >> 4) * 256 + (((lane & 15) ^ ((4 * i + (lane >> 4)) & 15)) << 4);
  {
    const unsigned a0 = (w & 1) ? vsw[2] : vsw[0], a1 = (w & 1) ? vsw[3] : vsw[1];
    DMA_PAIR(lds + LDS_C + w * 2048, omega + w * 2048, a0, a1, "");
  }
  const unsigned slotA = lds + LDS_A + w * 8192;
  DMA_TILE(slotA, k + (row0 + w * 32) * 256, vsw[0], vsw[1], vsw[2], vsw[3]);
  {
    const unsigned vv = (lane >> 4) * 256 + (((lane & 15) ^ ((lane >> 4) << 2)) << 4);
#pragma unroll
    for (int c = 0; c < 4; ++c) DMA_PAIR(lds + LDS_B + c * 16384 + w * 2048, v + row0 * 256 + c * 16384 + w * 2048, vv, vv, LD_POL);
  }
  asm volatile("s_waitcnt vmcnt(16)" : "+v"(ep)::"memory");
  const u64 tag = (((u64)__builtin_amdgcn_readfirstlane(ep[1]) << 32) | (unsigned)__builtin_amdgcn_readfirstlane(ep[0])) + 1;
  {
    const u32x2 t2 = {(unsigned)tag, (unsigned)(tag >> 32)};
    if (lane == 0) asm volatile("global_store_dwordx2 %0, %1, %2 sc1" ::"v"((unsigned)chunk * 8), "v"(t2), "s"(ctl) : "memory");
  }
  BARRIER();
  const unsigned xr = (h ^ (r & 15)) << 4;
  u32x4 om[2][8];
#pragma unroll
  for (int i = 0; i < 2; ++i)
#pragma unroll
    for (int t = 0; t < 8; ++t) om[i][t] = lds_rd128(lds + LDS_C + (32 * i + r) * 256 + (xr ^ (t << 5)));
  BARRIER();

  const float scale = 0.29730177875068026f;
  const float c1 = scale * 1.4426950408889634f;
  const float c2 = -0.5f * scale * scale * 1.4426950408889634f;

  u32x2 fl;
  {
    WAIT_VM(9);
    u32x4 kf[8];
#pragma unroll
    for (int t = 0; t < 8; ++t) kf[t] = lds_rd128(slotA + r * 256 + (xr ^ (t << 5)));
    asm volatile("s_waitcnt lgkmcnt(0)" : "+v"(kf[0]), "+v"(kf[1]), "+v"(kf[2]), "+v"(kf[3]), "+v"(kf[4]), "+v"(kf[5]), "+v"(kf[6]), "+v"(kf[7])::"memory");
    asm volatile("global_load_dwordx2 %0, %1, %2 sc1" : "=v"(fl) : "v"((lane & 7) * 8), "s"(ctl) : "memory");
    DMA_TILE(slotA, q + (row0 + w * 32) * 256, vsw[0], vsw[1], vsw[2], vsw[3]);
    f32x16 acc[2] = {};
    float ss = 0.f;
#pragma unroll
    for (int t = 0; t < 8; ++t) {
      acc[0] = MFMA(as_frag(kf[t]), as_frag(om[0][t]), acc[0]);
      acc[1] = MFMA(as_frag(kf[t]), as_frag(om[1][t]), acc[1]);
      ss = sumsq(kf[t], ss);
    }
    const float bias = add_halves(ss) * c2 - 3.0f;
    const unsigned sc = lds + LDS_C + w * 4096;
    *(LAS float*)(size_t)(sc + r * 4) = bias;
    f32x4 bz[4];
#pragma unroll
    for (int g = 0; g < 4; ++g) bz[g] = *(const LAS f32x4*)(size_t)(sc + (8 * g + 4 * h) * 4);
#pragma unroll
    for (int i = 0; i < 2; ++i)
#pragma unroll
      for (int s = 0; s < 2; ++s) {
        u32x4 f;
#pragma unroll
        for (int j = 0; j < 4; ++j) {
          const int e = 8 * s + 2 * j;
          const float p0 = __builtin_amdgcn_exp2f(acc[i][e] * c1 + bz[e >> 2][e & 3]) + 1e-6f;
          const float p1 = __builtin_amdgcn_exp2f(acc[i][e + 1] * c1 + bz[(e + 1) >> 2][(e + 1) & 3]) + 1e-6f;
          f[j] = pk_bf16(p0, p1);
        }
        lds_wr128(sc + (i * 2 + s) * 1024 + l16, f);
      }
  }
  BARRIER();

  f32x16 kv = {};
  {
    u32x4 af[16];
#pragma unroll
    for (int ks = 0; ks < 16; ++ks) af[ks] = lds_rd128(lds + LDS_C + (ks >> 1) * 4096 + (mt * 2 + (ks & 1)) * 1024 + l16);
    if (zwave) {
      const u32x4 ones = {0x3F803F80u, 0x3F803F80u, 0x3F803F80u, 0x3F803F80u};
      f32x16 za = {};
#pragma unroll
      for (int ks = 0; ks < 16; ++ks) za = MFMA(as_frag(ones), as_frag(af[ks]), za);
      const float z0 = za[0];
      const unsigned zz = min(__builtin_bit_cast(unsigned, z0), INVALID - 1);
      char* myz = zbuf + chunk * 128;
      if (lane < 32) asm volatile("global_store_dword %0, %1, %2 sc1" ::"v"(r * 4), "v"(zz), "s"(myz) : "memory");
    }
    const unsigned qq = (lane & 15) >> 2, p = lane & 3, blk = (lane >> 4) & 1;
    const unsigned trb = lds + LDS_B + (4 * h + qq) * 256 + (((4 * dt + 2 * blk + (p >> 1)) ^ (qq << 2)) << 4) + 8 * (p & 1);
#pragma unroll
    for (int c = 0; c < 4; ++c) {
      WAIT_VM(16 - 2 * c);
      BARRIER();
#pragma unroll
      for (int u = 0; u < 4; ++u) {
        const int ks = 4 * c + u;
        const s16x4 lo = __builtin_amdgcn_ds_read_tr16_b64_v4i16((LAS s16x4*)(size_t)(trb + (16 * ks) * 256));
        const s16x4 hi = __builtin_amdgcn_ds_read_tr16_b64_v4i16((LAS s16x4*)(size_t)(trb + (16 * ks + 8) * 256));
        const bf16x8 bf = __builtin_bit_cast(bf16x8, __builtin_shufflevector(lo, hi, 0, 1, 2, 3, 4, 5, 6, 7));
        kv = MFMA(as_frag(af[ks]), bf, kv);
      }
    }
  }
  {
    char* mykv = kvbuf + chunk * 2048;
    u32x4 d0, d1;
#pragma unroll
    for (int j = 0; j < 4; ++j) {
      d0[j] = min(pk_bf16(kv[2 * j], kv[2 * j + 1]), INVALID - 1);
      d1[j] = min(pk_bf16(kv[8 + 2 * j], kv[8 + 2 * j + 1]), INVALID - 1);
    }
    ST128_DEV(mykv, l16, d0, 0);
    ST128_DEV(mykv, l16, d1, 1024);
  }
  asm volatile("s_waitcnt vmcnt(10)" : "+v"(fl)::"memory");
  for (unsigned spin = 0; spin < SPIN_CAP; ++spin) {
    if (__builtin_amdgcn_ballot_w64(fl[0] != (unsigned)tag || fl[1] != (unsigned)(tag >> 32)) == 0) break;
    __builtin_amdgcn_s_sleep(2);
    asm volatile("global_load_dwordx2 %0, %1, %2 sc1\n\ts_waitcnt vmcnt(0)" : "=&v"(fl) : "v"((lane & 7) * 8), "s"(ctl) : "memory");
  }
  if (chunk == 0) {
    const u32x2 t2 = {(unsigned)tag, (unsigned)(tag >> 32)};
    if (lane == 0) asm volatile("global_store_dwordx2 %0, %1, %2 offset:64 sc1" ::"v"(0u), "v"(t2), "s"(ctl) : "memory");
  }

  WAIT_VM(9 - QWAIT);
  u32x4 zl = {0u, 0u, 0u, 0u};
  if (zwave) LD128_DEV(zl, zbuf, l16, 0);
  u32x4 pa[7], pb[7];
#pragma unroll
  for (int i = 0; i < 7; ++i) {
    const char* src = kvbuf + ((chunk + 1 + i) & 7) * 2048;
    LD128_DEV(pa[i], src, l16, 0);
    LD128_DEV(pb[i], src, l16, 1024);
  }
  u32x4 xq[2][2];
  {
    WAIT_VM(14);
    u32x4 qf[8];
#pragma unroll
    for (int t = 0; t < 8; ++t) qf[t] = lds_rd128(slotA + r * 256 + (xr ^ (t << 5)));
    f32x16 acc[2] = {};
    float ss = 0.f;
#pragma unroll
    for (int t = 0; t < 8; ++t) {
      acc[0] = MFMA(as_frag(om[0][t]), as_frag(qf[t]), acc[0]);
      acc[1] = MFMA(as_frag(om[1][t]), as_frag(qf[t]), acc[1]);
      ss = sumsq(qf[t], ss);
    }
    const float bias = add_halves(ss) * c2 - 3.0f;
#pragma unroll
    for (int i = 0; i < 2; ++i)
#pragma unroll
      for (int s = 0; s < 2; ++s)
#pragma unroll
        for (int j = 0; j < 4; ++j) {
          const int e = 8 * s + 2 * j;
          xq[i][s][j] = pk_bf16(__builtin_amdgcn_exp2f(acc[i][e] * c1 + bias) + 1e-6f, __builtin_amdgcn_exp2f(acc[i][e + 1] * c1 + bias) + 1e-6f);
        }
  }
  {
    u32x4 id[2];
#pragma unroll
    for (int s = 0; s < 2; ++s)
#pragma unroll
      for (int j = 0; j < 4; ++j) {
        const unsigned k0 = 16 * s + 8 * (j >> 1) + 4 * h + 2 * (j & 1);
        id[s][j] = (r == k0 ? 0x3F80u : 0u) | (r == k0 + 1 ? 0x3F800000u : 0u);
      }
    unsigned pend = 0;
#pragma unroll
    for (int i = 0; i < 7; ++i) {
      asm volatile("s_waitcnt vmcnt(%2)" : "+v"(pa[i]), "+v"(pb[i]) : "n"(12 - 2 * i) : "memory");
      if (words_valid(pa[i], pb[i])) {
        kv = MFMA(as_frag(id[0]), as_frag(pa[i]), kv);
        kv = MFMA(as_frag(id[1]), as_frag(pb[i]), kv);
      } else pend |= 1u << i;
    }
    asm volatile("" : "+v"(zl));
    bool zok = !zwave || words_valid(zl, zl);
    for (unsigned spin = 0; (pend || !zok) && spin < SPIN_CAP; ++spin) {
      LD128_DEV(zl, zbuf, l16, 0);
#pragma unroll
      for (int i = 0; i < 7; ++i) {
        const char* src = kvbuf + ((chunk + 1 + i) & 7) * 2048;
        LD128_DEV(pa[i], src, l16, 0);
        LD128_DEV(pb[i], src, l16, 1024);
      }
      asm volatile("s_waitcnt vmcnt(0)" : "+v"(zl), "+v"(pa[0]), "+v"(pb[0]), "+v"(pa[1]), "+v"(pb[1]), "+v"(pa[2]), "+v"(pb[2]), "+v"(pa[3]), "+v"(pb[3]),
                   "+v"(pa[4]), "+v"(pb[4]), "+v"(pa[5]), "+v"(pb[5]), "+v"(pa[6]), "+v"(pb[6])::"memory");
#pragma unroll
      for (int i = 0; i < 7; ++i)
        if ((pend >> i & 1) && words_valid(pa[i], pb[i])) {
          kv = MFMA(as_frag(id[0]), as_frag(pa[i]), kv);
          kv = MFMA(as_frag(id[1]), as_frag(pb[i]), kv);
          pend &= ~(1u << i);
        }
      zok = !zwave || words_valid(zl, zl);
    }
    u32x4 d0, d1;
#pragma unroll
    for (int j = 0; j < 4; ++j) {
      d0[j] = pk_bf16(kv[2 * j], kv[2 * j + 1]);
      d1[j] = pk_bf16(kv[8 + 2 * j], kv[8 + 2 * j + 1]);
    }
    lds_wr128(lds + LDS_C + w * 2048 + l16, d0);
    lds_wr128(lds + LDS_C + w * 2048 + 1024 + l16, d1);
    if (zwave) {
      float z4[4];
#pragma unroll
      for (int i = 0; i < 4; ++i) {
        const unsigned zu = zl[i];
        float x = __builtin_bit_cast(float, zu);
        x += __shfl_xor(x, 8);
        x += __shfl_xor(x, 16);
        z4[i] = add_halves(x);
      }
      const unsigned qd = lane & 7;
      const u32x2 zp = {pk_bf16(z4[0], z4[1]), pk_bf16(z4[2], z4[3])};
      if (lane < 8) lds_wr64(lds + LDS_Z + mt * 64 + (((qd >> 2) * 2 + (qd & 1)) * 2 + ((qd >> 1) & 1)) * 8, zp);
    }
  }
  BARRIER();

  {
    f32x16 den = {};
#pragma unroll
    for (int i = 0; i < 2; ++i)
#pragma unroll
      for (int s = 0; s < 2; ++s) den = MFMA(as_frag(lds_rd128(lds + LDS_Z + i * 64 + (s * 2 + h) * 16)), as_frag(xq[i][s]), den);
    const float inv = __builtin_amdgcn_rcpf(den[0] + 1e-6f);
    const unsigned wb = slotA + r * 256 + 8 * (h ^ ((r >> 3) & 1));
#pragma unroll
    for (int d = 0; d < 4; ++d) {
      f32x16 o = {};
#pragma unroll
      for (int i = 0; i < 2; ++i)
#pragma unroll
        for (int s = 0; s < 2; ++s) o = MFMA(as_frag(lds_rd128(lds + LDS_C + (i * 4 + d) * 2048 + s * 1024 + l16)), as_frag(xq[i][s]), o);
#pragma unroll
      for (int g = 0; g < 4; ++g) {
        const u32x2 pk = {pk_bf16(o[4 * g] * inv, o[4 * g + 1] * inv), pk_bf16(o[4 * g + 2] * inv, o[4 * g + 3] * inv)};
        lds_wr64(wb + (((4 * d + g) ^ (r & 7)) << 4), pk);
      }
    }
    char* dst = out + (row0 + w * 32) * 256 + 4096;
#pragma unroll
    for (int i = 0; i < 8; ++i) {
      const unsigned row = 4 * i + (lane >> 4);
      u32x4 x = lds_rd128(slotA + row * 256 + (((lane & 15) ^ (row & 7)) << 4));
      if ((i >> 1) & 1) x = (u32x4){x[2], x[3], x[0], x[1]};
      asm volatile("global_store_dwordx4 %0, %1, %2 offset:%3 " ST_POL "\n\ts_nop 1" ::"v"(l16), "v"(x), "s"(dst), "n"(i * 1024 - 4096) : "memory");
    }
  }
}

extern "C" void kernel_launch(void* const* d_in, const int* in_sizes, int n_in, void* d_out, int out_size, void* d_ws,
                              size_t ws_size, hipStream_t stream) {
  rfa_kernel<<<256, 512, 0, stream>>>((const char*)d_in[0], (const char*)d_in[1], (const char*)d_in[2], (const char*)d_in[3],
                                      (char*)d_out, (char*)d_ws);
}
```

```cpp
#include <hip/hip_runtime.h>
#include <hip/hip_bf16.h>
#include <stdint.h>

typedef __bf16 bf16x8 __attribute__((ext_vector_type(8)));
typedef __bf16 bf16x2 __attribute__((ext_vector_type(2)));
typedef short s16x4 __attribute__((ext_vector_type(4)));
typedef float f32x16 __attribute__((ext_vector_type(16)));
typedef float f32x4 __attribute__((ext_vector_type(4)));
typedef unsigned u32x4 __attribute__((ext_vector_type(4)));
typedef unsigned u32x2 __attribute__((ext_vector_type(2)));
#define LAS __attribute__((address_space(3)))
#define DI __device__ __forceinline__

#define LD_POL "nt"
#define WIN 24
#define PRO 40
#define GATE 16
#define ROT 1

constexpr int S = 2048;
constexpr int LDS_A = 0;
constexpr int LDS_B = 65536;
constexpr int LDS_OM = LDS_B + 49152;
constexpr int LDS_C = 131072;
constexpr int LDS_D = 155648;
constexpr int W_LANDED = LDS_D, W_OM = LDS_D + 4, W_PHI = LDS_D + 8, W_AF = LDS_D + 12, W_KV = LDS_D + 16, W_H = LDS_D + 20;
constexpr int LDS_Z = LDS_D + 128, LDS_ID = LDS_D + 256;
constexpr int P_K = 16, P_V = 80, P_Q = 144, P_END = 208;
constexpr size_t WS_EP = 0;
constexpr size_t WS_AUX = 65536;
constexpr size_t WS_KV = WS_AUX + 1048576;
constexpr unsigned SPIN_CAP = 1u << 18;

#define STAMP(i)
#define COUNT_RETRY()
#define MFMA(a, b, c) __builtin_amdgcn_mfma_f32_32x32x16_bf16((a), (b), (c), 0, 0, 0)
#define WAIT_VM(n) asm volatile("s_waitcnt vmcnt(%0)" ::"n"(n) : "memory")
#define SCHED_FENCE() __builtin_amdgcn_sched_barrier(0)

DI unsigned pk_bf16(float lo, float hi) { bf16x2 p; p[0] = (__bf16)lo; p[1] = (__bf16)hi; return __builtin_bit_cast(unsigned, p); }
DI bf16x8 as_frag(u32x4 v) { return __builtin_bit_cast(bf16x8, v); }
DI u32x4 lds_rd128(unsigned a) { return *(const LAS u32x4*)(size_t)a; }
DI void lds_wr128(unsigned a, u32x4 v) { *(LAS u32x4*)(size_t)a = v; }
DI void lds_wr64(unsigned a, u32x2 v) { *(LAS u32x2*)(size_t)a = v; }
DI float dot2(unsigned a, unsigned b, float c) { return __builtin_amdgcn_fdot2_f32_bf16(__builtin_bit_cast(bf16x2, a), __builtin_bit_cast(bf16x2, b), c, false); }
DI float add_halves(float x) {
  const unsigned u = __builtin_bit_cast(unsigned, x);
  const auto r = __builtin_amdgcn_permlane32_swap(u, u, false, false);
  return __builtin_bit_cast(float, (unsigned)r[0]) + __builtin_bit_cast(float, (unsigned)r[1]);
}
DI void pack_tile(const f32x16& t, u32x4& d0, u32x4& d1) {
#pragma unroll
  for (int j = 0; j < 4; ++j) { d0[j] = pk_bf16(t[2 * j], t[2 * j + 1]); d1[j] = pk_bf16(t[8 + 2 * j], t[8 + 2 * j + 1]); }
}
DI unsigned rot(unsigned x, int k) { return ROT ? __builtin_amdgcn_alignbit(x, x, k) : x; }
DI unsigned check_word(u32x4 a, u32x4 b, unsigned z, unsigned salt) {
  return ((a[0] + rot(a[1], 3) + rot(a[2], 5)) + (rot(a[3], 7) + rot(b[0], 9) + rot(b[1], 11))) + ((rot(b[2], 13) + rot(b[3], 15) + z) + salt);
}

#define DMA_QUAD(lds, g, v0, v1, v2, v3, POL)                                                   \
  asm volatile("s_mov_b32 m0, %0\n\ts_nop 0\n\t"                                                \
               "global_load_lds_dwordx4 %2, %1 offset:0 " POL "\n\t"                            \
               "global_load_lds_dwordx4 %3, %1 offset:1024 " POL "\n\t"                         \
               "global_load_lds_dwordx4 %4, %1 offset:2048 " POL "\n\t"                         \
               "global_load_lds_dwordx4 %5, %1 offset:3072 " POL                                \
               ::"s"(lds), "s"(g), "v"(v0), "v"(v1), "v"(v2), "v"(v3) : "memory")
#define ST128_DEV(g, voff, val, off)                                                            \
  asm volatile("global_store_dwordx4 %0, %1, %2 offset:%3 sc1\n\ts_nop 1" ::"v"(voff), "v"(val), "s"(g), "n"(off) : "memory")

DI void wait_ge(unsigned addr, unsigned need) {
  for (;;) {
    unsigned x;
    asm volatile("ds_read_b32 %0, %1\n\ts_waitcnt lgkmcnt(0)" : "=v"(x) : "v"(addr) : "memory");
    if ((unsigned)__builtin_amdgcn_readfirstlane(x) >= need) break;
    __builtin_amdgcn_s_sleep(1);
  }
}
DI void arrive(unsigned addr) {
  unsigned long long ex;
  asm volatile("s_mov_b64 %0, exec\n\ts_mov_b64 exec, 1\n\tds_add_u32 %1, %2\n\ts_mov_b64 exec, %0" : "=&s"(ex) : "v"(addr), "v"(1u) : "memory");
}
DI void post(unsigned addr, unsigned val) { asm volatile("ds_write_b32 %0, %1" ::"v"(addr), "v"(val) : "memory"); }

template <bool B> struct BC { static constexpr bool value = B; };

__global__ __launch_bounds__(576, 3) void rfa_kernel(const char* __restrict__ q, const char* __restrict__ k,
                                                     const char* __restrict__ v, const char* __restrict__ omega,
                                                     char* __restrict__ out, char* ws) {
  __shared__ __attribute__((aligned(16))) char smem[163840];
  STAMP(0);
  const unsigned lds = (unsigned)(size_t)(LAS char*)smem;
  const int w = __builtin_amdgcn_readfirstlane(threadIdx.x >> 6);
  const unsigned lane = threadIdx.x & 63, r = lane & 31, h = lane >> 5;
  const int b = blockIdx.x, head = (b & 7) * 4 + (b >> 6), chunk = (b >> 3) & 7;
  const size_t row0 = (size_t)head * S + chunk * 256;
  const unsigned l16 = lane * 16;
  unsigned vs[4];
#pragma unroll
  for (int i = 0; i < 4; ++i) vs[i] = (lane >> 4) * 256 + (((lane & 15) ^ ((4 * i + (lane >> 4)) & 15)) << 4);
  const char* kk = k + row0 * 256;
  if (w == 8) {
#pragma unroll
    for (int pos = 0; pos < PRO; pos += 4) {
      if (pos < P_K) DMA_QUAD(lds + LDS_OM + pos * 1024, omega + pos * 1024, vs[0], vs[1], vs[2], vs[3], "");
      else DMA_QUAD(lds + LDS_A + (pos - P_K) * 1024, kk + (pos - P_K) * 1024, vs[0], vs[1], vs[2], vs[3], LD_POL);
    }
    post(lds + LDS_D + (lane & 7) * 4, 0u);
  }
  if (w == 7) {
    const unsigned pos = ((r >> 2) & 1) == h ? ((r >> 4) << 2) | (((r >> 3) & 1) << 1) | ((r >> 1) & 1) : 8u;
    const unsigned one = (r & 1) ? 0x3F800000u : 0x3F80u;
    u32x4 id0, id1;
#pragma unroll
    for (int j = 0; j < 4; ++j) { id0[j] = pos == j ? one : 0u; id1[j] = pos == 4 + j ? one : 0u; }
    lds_wr128(lds + LDS_ID + l16, id0);
    lds_wr128(lds + LDS_ID + 1024 + l16, id1);
  }
  asm volatile("s_waitcnt lgkmcnt(0)\n\ts_barrier" ::: "memory");

  if (w == 8) {
    const unsigned vv = (lane >> 4) * 256 + (((lane & 15) ^ ((lane >> 4) << 2)) << 4);
    const char* vsrc = v + row0 * 256;
    const char* qq = q + row0 * 256;
#pragma unroll
    for (int n = PRO - 16; n > WIN - 4; n -= 4) {
      WAIT_VM(n);
      post(lds + W_LANDED, PRO - n);
    }
#pragma unroll
    for (int pos = PRO; pos < P_END; pos += 4) {
      if (pos == P_V + 48) wait_ge(lds + W_OM, 8);
      if (pos == P_Q + 24) wait_ge(lds + W_AF, 8);
      if (pos < P_V) DMA_QUAD(lds + LDS_A + (pos - P_K) * 1024, kk + (pos - P_K) * 1024, vs[0], vs[1], vs[2], vs[3], LD_POL);
      else if (pos < P_Q) DMA_QUAD(lds + LDS_B + (pos - P_V) * 1024, vsrc + (pos - P_V) * 1024, vv, vv, vv, vv, LD_POL);
      else {
        const int o = (pos - P_Q) * 1024;
        DMA_QUAD(lds + (o < 24576 ? LDS_C + o : LDS_A + o - 24576), qq + o, vs[0], vs[1], vs[2], vs[3], LD_POL);
      }
      WAIT_VM(WIN);
      post(lds + W_LANDED, pos + 4 - WIN);
      if (pos + 4 == P_V) STAMP(1);
      if (pos + 4 == P_Q) STAMP(2);
    }
    STAMP(3);
#pragma unroll
    for (int n = WIN - 4; n >= 0; n -= 4) {
      WAIT_VM(n);
      post(lds + W_LANDED, P_END - n);
    }
    STAMP(4);
    return;
  }

  const int mt = (w >> 1) & 1, dt = (w & 1) | ((w >> 2) << 1);
  const bool zwave = dt == 0;
  char* epw = ws + WS_EP + head * 128;
  char* auxbuf = ws + WS_AUX + (size_t)(head * 8 + w) * 4096;
  char* kvbuf = ws + WS_KV + (size_t)(head * 8 + w) * 16384;
  __builtin_amdgcn_s_setprio(1);
  unsigned ep;
  asm volatile("global_load_dword %0, %1, %2 sc1" : "=v"(ep) : "v"(0u), "s"(epw) : "memory");
  STAMP(1);

  const unsigned xr = r * 256 + ((h ^ (r & 15)) << 4);
  u32x4 om[2][8];
  wait_ge(lds + W_LANDED, P_K);
#pragma unroll
  for (int i = 0; i < 2; ++i)
#pragma unroll
    for (int t = 0; t < 8; ++t) om[i][t] = lds_rd128(((lds + LDS_OM + xr) ^ (t << 5)) + i * 8192);
  arrive(lds + W_OM);

  const float scale = 0.29730177875068026f;
  const float c1 = scale * 1.4426950408889634f;
  const float c2 = -0.5f * scale * scale * 1.4426950408889634f;

  {
    const unsigned slotA = lds + LDS_A + w * 8192;
    wait_ge(lds + W_LANDED, P_K + 8 * (w + 1));
    u32x4 kf[8];
    unsigned ka = slotA + xr;
    asm volatile("" : "+v"(ka));
#pragma unroll
    for (int t = 0; t < 8; ++t) kf[t] = lds_rd128(ka ^ (t << 5));
    SCHED_FENCE();
    f32x16 acc[2] = {};
    float ss[4] = {};
#pragma unroll
    for (int t = 0; t < 8; ++t)
#pragma unroll
      for (int i = 0; i < 4; ++i) ss[i] = dot2(kf[t][i], kf[t][i], ss[i]);
#pragma unroll
    for (int t = 0; t < 8; ++t) acc[0] = MFMA(as_frag(kf[t]), as_frag(om[0][t]), acc[0]);
    const float bias = add_halves((ss[0] + ss[1]) + (ss[2] + ss[3])) * c2 - 3.0f;
    *(LAS float*)(size_t)(slotA + 4096 + r * 4) = bias;
    f32x4 bz[4];
#pragma unroll
    for (int g = 0; g < 4; ++g) bz[g] = *(const LAS f32x4*)(size_t)(slotA + 4096 + (8 * g + 4 * h) * 4);
#pragma unroll
    for (int t = 0; t < 8; ++t) acc[1] = MFMA(as_frag(kf[t]), as_frag(om[1][t]), acc[1]);
#pragma unroll
    for (int i = 0; i < 2; ++i)
#pragma unroll
      for (int s = 0; s < 2; ++s) {
        u32x4 f;
#pragma unroll
        for (int j = 0; j < 4; ++j) {
          const int e = 8 * s + 2 * j;
          const float p0 = __builtin_amdgcn_exp2f(acc[i][e] * c1 + bz[e >> 2][e & 3]) + 1e-6f;
          const float p1 = __builtin_amdgcn_exp2f(acc[i][e + 1] * c1 + bz[(e + 1) >> 2][(e + 1) & 3]) + 1e-6f;
          f[j] = pk_bf16(p0, p1);
        }
        lds_wr128(slotA + (i * 2 + s) * 1024 + l16, f);
      }
  }
  arrive(lds + W_PHI);
  asm volatile("s_waitcnt vmcnt(0)" : "+v"(ep)::"memory");
  const unsigned tag = __builtin_amdgcn_readfirstlane(ep) + 1;
  const unsigned salt = tag + lane * 0x9E3779B1u;
  wait_ge(lds + W_PHI, 8);
  STAMP(4);

  f32x16 kv = {};
  float zown = 0.f;
  {
    const unsigned ab = lds + LDS_A + mt * 2048 + l16;
    u32x4 af[16];
#pragma unroll
    for (int ks = 0; ks < 10; ++ks) af[ks] = lds_rd128(ab + (ks >> 1) * 8192 + (ks & 1) * 1024);
    arrive(lds + W_AF);
    float zs[4] = {};
    const unsigned qq = (lane & 15) >> 2, p = lane & 3, blk = (lane >> 4) & 1;
    const unsigned trb = lds + LDS_B + (4 * h + qq) * 256 + (((4 * dt + 2 * blk + (p >> 1)) ^ (qq << 2)) << 4) + 8 * (p & 1);
#pragma unroll
    for (int c = 0; c < 4; ++c) {
      if (c == 1 || c == 2) {
#pragma unroll
        for (int ks = (c == 1 ? 10 : 12); ks < (c == 1 ? 12 : 16); ++ks) af[ks] = lds_rd128(ab + (ks >> 1) * 8192 + (ks & 1) * 1024);
      }
      if (zwave && c < 3) {
#pragma unroll
        for (int ks = (c == 0 ? 0 : 4 * c + 4); ks < 4 * c + 8; ++ks)
#pragma unroll
          for (int i = 0; i < 4; ++i) zs[i] = dot2(af[ks][i], 0x3F803F80u, zs[i]);
        if (c == 2) zown = add_halves((zs[0] + zs[1]) + (zs[2] + zs[3]));
      }
      SCHED_FENCE();
      wait_ge(lds + W_LANDED, P_V + 16 * (c + 1));
      if (c == 3) STAMP(12);
      s16x4 vf[8];
#pragma unroll
      for (int u = 0; u < 8; ++u) vf[u] = __builtin_amdgcn_ds_read_tr16_b64_v4i16((LAS s16x4*)(size_t)(trb + (64 * c + 8 * u) * 256));
      SCHED_FENCE();
#pragma unroll
      for (int u = 0; u < 4; ++u)
        kv = MFMA(as_frag(af[4 * c + u]), __builtin_bit_cast(bf16x8, __builtin_shufflevector(vf[2 * u], vf[2 * u + 1], 0, 1, 2, 3, 4, 5, 6, 7)), kv);
    }
    arrive(lds + W_KV);
  }
  {
    u32x4 d0, d1;
    pack_tile(kv, d0, d1);
    ST128_DEV(kvbuf + chunk * 2048, l16, d0, 0);
    ST128_DEV(kvbuf + chunk * 2048, l16, d1, 1024);
    const unsigned zb = __builtin_bit_cast(unsigned, zown);
    const u32x2 ax = {check_word(d0, d1, zb, salt), zb};
    if (zwave) asm volatile("global_store_dwordx2 %0, %1, %2 sc1" ::"v"(lane * 8), "v"(ax), "s"(auxbuf + chunk * 512) : "memory");
    else asm volatile("global_store_dword %0, %1, %2 sc1" ::"v"(lane * 4), "v"(ax[0]), "s"(auxbuf + chunk * 512) : "memory");
  }
  STAMP(5);

  if (w >= 4) __builtin_amdgcn_s_setprio(2);
  const unsigned slotQ = w < 3 ? lds + LDS_C + w * 8192 : lds + LDS_A + (w - 3) * 8192;
  u32x4 xq[4];
  {
    wait_ge(lds + W_LANDED, P_Q + 8 * (w + 1));
    STAMP(7);
    u32x4 qf[8];
    unsigned qa = slotQ + xr;
    asm volatile("" : "+v"(qa));
#pragma unroll
    for (int t = 0; t < 8; ++t) qf[t] = lds_rd128(qa ^ (t << 5));
    SCHED_FENCE();
    f32x16 acc[2] = {};
    float ss[4] = {};
#pragma unroll
    for (int t = 0; t < 8; ++t)
#pragma unroll
      for (int i = 0; i < 4; ++i) ss[i] = dot2(qf[t][i], qf[t][i], ss[i]);
#pragma unroll
    for (int t = 0; t < 8; ++t) acc[0] = MFMA(as_frag(om[0][t]), as_frag(qf[t]), acc[0]);
    const float bias = add_halves((ss[0] + ss[1]) + (ss[2] + ss[3])) * c2 - 3.0f;
#pragma unroll
    for (int t = 0; t < 8; ++t) acc[1] = MFMA(as_frag(om[1][t]), as_frag(qf[t]), acc[1]);
#pragma unroll
    for (int u = 0; u < 4; ++u)
#pragma unroll
      for (int j = 0; j < 4; ++j) {
        const int e = 8 * (u & 1) + 2 * j;
        xq[u][j] = pk_bf16(__builtin_amdgcn_exp2f(acc[u >> 1][e] * c1 + bias) + 1e-6f, __builtin_amdgcn_exp2f(acc[u >> 1][e + 1] * c1 + bias) + 1e-6f);
      }
    asm volatile("" : "+v"(xq[0]), "+v"(xq[1]), "+v"(xq[2]), "+v"(xq[3]));
    SCHED_FENCE();
  }
  unsigned wb = slotQ + r * 256 + 8 * (h ^ ((r >> 3) & 1)), wx = (r & 7) << 4;
  char* dst = out + (row0 + w * 32) * 256;
  unsigned so = (lane >> 3) * 256 + (lane & 7) * 16;
  unsigned rb = slotQ + (lane >> 3) * 256 + (((lane & 7) ^ (lane >> 3)) << 4);
  unsigned kb = lds + LDS_B + l16, zb = lds + LDS_Z + h * 16;
  asm volatile("" : "+v"(wb), "+v"(wx), "+v"(so), "+v"(rb), "+v"(kb), "+v"(zb), "+s"(dst));

  __builtin_amdgcn_s_setprio(1);
  wait_ge(lds + W_KV, 8);
  wait_ge(lds + W_LANDED, P_END - GATE);
  STAMP(6);
  auto gather = [&](auto with_z) {
    constexpr bool Z = decltype(with_z)::value;
    u32x4 pa[7], pb[7];
    unsigned pc[7];
    u32x2 px[7];
#define REC_LOAD(i, TAIL, C)                                                                                                          \
  do { const int c_ = (chunk + 1 + (i)) & 7;                                                                                          \
       if (Z) asm volatile("global_load_dwordx4 %0, %3, %5 sc1\n\tglobal_load_dwordx4 %1, %3, %5 offset:1024 sc1\n\t"                 \
                           "global_load_dwordx2 %2, %4, %6 sc1" TAIL : C(pa[i]), C(pb[i]), C(px[i])                                   \
                           : "v"(l16), "v"(lane * 8), "s"(kvbuf + c_ * 2048), "s"(auxbuf + c_ * 512) : "memory");                     \
       else asm volatile("global_load_dwordx4 %0, %3, %5 sc1\n\tglobal_load_dwordx4 %1, %3, %5 offset:1024 sc1\n\t"                   \
                         "global_load_dword %2, %4, %6 sc1" TAIL : C(pa[i]), C(pb[i]), C(pc[i])                                       \
                         : "v"(l16), "v"(lane * 4), "s"(kvbuf + c_ * 2048), "s"(auxbuf + c_ * 512) : "memory"); } while (0)
#define REC_BAD(i) (__builtin_amdgcn_ballot_w64(check_word(pa[i], pb[i], Z ? px[i][1] : 0u, salt) != (Z ? px[i][0] : pc[i])) != 0)
#pragma unroll
    for (int i = 0; i < 7; ++i) REC_LOAD(i, "", "=&v");
    if (w >= 4) __builtin_amdgcn_s_setprio(2);
    STAMP(14);
    const u32x4 id0 = lds_rd128(lds + LDS_ID + l16), id1 = lds_rd128(lds + LDS_ID + 1024 + l16);
    unsigned spin = 0;
#pragma unroll
    for (int i = 0; i < 7; ++i) {
      if (Z) asm volatile("s_waitcnt vmcnt(%3)" : "+v"(pa[i]), "+v"(pb[i]), "+v"(px[i]) : "n"(18 - 3 * i) : "memory");
      else asm volatile("s_waitcnt vmcnt(%3)" : "+v"(pa[i]), "+v"(pb[i]), "+v"(pc[i]) : "n"(18 - 3 * i) : "memory");
      if (__builtin_expect(REC_BAD(i), 0)) {
        do {
          COUNT_RETRY();
          __builtin_amdgcn_s_sleep(2);
          REC_LOAD(i, "\n\ts_waitcnt vmcnt(0)", "+v");
        } while (REC_BAD(i) && ++spin < SPIN_CAP);
      }
      kv = MFMA(as_frag(id0), as_frag(pa[i]), kv);
      kv = MFMA(as_frag(id1), as_frag(pb[i]), kv);
      if (Z) { const unsigned zu = px[i][1]; zown += __builtin_bit_cast(float, zu); }
    }
    STAMP(8);
    if (Z && lane < 32)
      *(LAS unsigned short*)(size_t)(lds + LDS_Z + mt * 64 + (((r >> 4) * 2 + ((r >> 2) & 1)) * 8 + ((r >> 3) & 1) * 4 + (r & 3)) * 2) = (unsigned short)pk_bf16(zown, 0.f);
    u32x4 d0, d1;
    pack_tile(kv, d0, d1);
    lds_wr128(kb + (mt * 4 + dt) * 2048, d0);
    lds_wr128(kb + (mt * 4 + dt) * 2048 + 1024, d1);
    arrive(lds + W_H + (w >> 2) * 4);
  };
  if (zwave) gather(BC<true>{}); else gather(BC<false>{});
  STAMP(9);

  {
    float inv;
#pragma unroll
    for (int half = 0; half < 2; ++half) {
      wait_ge(lds + W_H + half * 4, 4);
      if (half) STAMP(3); else STAMP(10);
      u32x4 zf[4], kf2[2][4];
      if (half == 0) {
#pragma unroll
        for (int u = 0; u < 4; ++u) zf[u] = lds_rd128(zb + u * 32);
      }
#pragma unroll
      for (int d = 0; d < 2; ++d) {
        SCHED_FENCE();
#pragma unroll
        for (int u = 0; u < 4; ++u) kf2[d][u] = lds_rd128(kb + ((u >> 1) * 4 + 2 * half + d) * 2048 + (u & 1) * 1024);
      }
      SCHED_FENCE();
      f32x16 den = {}, o[2] = {};
      if (half == 0) {
#pragma unroll
        for (int u = 0; u < 4; ++u) den = MFMA(as_frag(zf[u]), as_frag(xq[u]), den);
      }
#pragma unroll
      for (int d = 0; d < 2; ++d)
#pragma unroll
        for (int u = 0; u < 4; ++u) o[d] = MFMA(as_frag(kf2[d][u]), as_frag(xq[u]), o[d]);
      if (half == 0) inv = __builtin_amdgcn_rcpf(den[0] + 1e-6f);
#pragma unroll
      for (int d = 0; d < 2; ++d)
#pragma unroll
        for (int g = 0; g < 4; ++g) {
          const u32x2 pk = {pk_bf16(o[d][4 * g] * inv, o[d][4 * g + 1] * inv), pk_bf16(o[d][4 * g + 2] * inv, o[d][4 * g + 3] * inv)};
          lds_wr64(wb + (((8 * half + 4 * d + g) << 4) ^ wx), pk);
        }
      u32x4 x[4];
#pragma unroll
      for (int i = 0; i < 4; ++i) x[i] = lds_rd128(rb + i * 2048 + half * 128);
#pragma unroll
      for (int i = 0; i < 4; ++i) {
        if (i & 1) x[i] = (u32x4){x[i][2], x[i][3], x[i][0], x[i][1]};
        ST128_DEV(dst, so + (i >> 1) * 4096, x[i], (i & 1) * 2048 + half * 128);
      }
      if (half == 0) STAMP(2);
    }
  }
  if (chunk == 0 && w == 0 && lane == 0)
    asm volatile("global_store_dword %0, %1, %2 sc1" ::"v"(0u), "v"(tag), "s"(epw) : "memory");
  STAMP(11);
}

extern "C" void kernel_launch(void* const* d_in, const int* in_sizes, int n_in, void* d_out, int out_size, void* d_ws,
                              size_t ws_size, hipStream_t stream) {
  rfa_kernel<<<256, 576, 0, stream>>>((const char*)d_in[0], (const char*)d_in[1], (const char*)d_in[2], (const char*)d_in[3],
                                      (char*)d_out, (char*)d_ws);
}
```

```cpp
#define HOLD_AT 11
#define HOLD_LDR 0
#include <hip/hip_runtime.h>
#include <hip/hip_bf16.h>
#include <stdint.h>

typedef __bf16 bf16x8 __attribute__((ext_vector_type(8)));
typedef __bf16 bf16x2 __attribute__((ext_vector_type(2)));
typedef short s16x4 __attribute__((ext_vector_type(4)));
typedef float f32x16 __attribute__((ext_vector_type(16)));
typedef float f32x4 __attribute__((ext_vector_type(4)));
typedef unsigned u32x4 __attribute__((ext_vector_type(4)));
typedef unsigned u32x2 __attribute__((ext_vector_type(2)));
#define LAS __attribute__((address_space(3)))
#define DI __device__ __forceinline__

#define LD_POL "nt"
#define WIN 24
#define PRO 40
#define GATE 16
#define ROT 1

constexpr int S = 2048;
constexpr int LDS_A = 0;
constexpr int LDS_B = 65536;
constexpr int LDS_OM = LDS_B + 49152;
constexpr int LDS_C = 131072;
constexpr int LDS_D = 155648;
constexpr int W_LANDED = LDS_D, W_OM = LDS_D + 4, W_PHI = LDS_D + 8, W_AF = LDS_D + 12, W_KV = LDS_D + 16, W_H = LDS_D + 20;
constexpr int LDS_Z = LDS_D + 128, LDS_ID = LDS_D + 256;
constexpr int P_K = 16, P_V = 80, P_Q = 144, P_END = 208;
constexpr size_t WS_EP = 0;
constexpr size_t WS_AUX = 65536;
constexpr size_t WS_KV = WS_AUX + 1048576;
constexpr unsigned SPIN_CAP = 1u << 18;

#define STAMP(i) do { if ((i) == 0) hold0_ = __builtin_amdgcn_s_memrealtime(); if ((i) == HOLD_AT && (threadIdx.x < 512) == (HOLD_LDR == 0)) { while (__builtin_amdgcn_s_memrealtime() - hold0_ < 3000ull) __builtin_amdgcn_s_sleep(1); } } while (0)
#define COUNT_RETRY()
#define MFMA(a, b, c) __builtin_amdgcn_mfma_f32_32x32x16_bf16((a), (b), (c), 0, 0, 0)
#define WAIT_VM(n) asm volatile("s_waitcnt vmcnt(%0)" ::"n"(n) : "memory")
#define SCHED_FENCE() __builtin_amdgcn_sched_barrier(0)

DI unsigned pk_bf16(float lo, float hi) { bf16x2 p; p[0] = (__bf16)lo; p[1] = (__bf16)hi; return __builtin_bit_cast(unsigned, p); }
DI bf16x8 as_frag(u32x4 v) { return __builtin_bit_cast(bf16x8, v); }
DI u32x4 lds_rd128(unsigned a) { return *(const LAS u32x4*)(size_t)a; }
DI void lds_wr128(unsigned a, u32x4 v) { *(LAS u32x4*)(size_t)a = v; }
DI void lds_wr64(unsigned a, u32x2 v) { *(LAS u32x2*)(size_t)a = v; }
DI float dot2(unsigned a, unsigned b, float c) { return __builtin_amdgcn_fdot2_f32_bf16(__builtin_bit_cast(bf16x2, a), __builtin_bit_cast(bf16x2, b), c, false); }
DI float add_halves(float x) {
  const unsigned u = __builtin_bit_cast(unsigned, x);
  const auto r = __builtin_amdgcn_permlane32_swap(u, u, false, false);
  return __builtin_bit_cast(float, (unsigned)r[0]) + __builtin_bit_cast(float, (unsigned)r[1]);
}
DI void pack_tile(const f32x16& t, u32x4& d0, u32x4& d1) {
#pragma unroll
  for (int j = 0; j < 4; ++j) { d0[j] = pk_bf16(t[2 * j], t[2 * j + 1]); d1[j] = pk_bf16(t[8 + 2 * j], t[8 + 2 * j + 1]); }
}
DI unsigned rot(unsigned x, int k) { return ROT ? __builtin_amdgcn_alignbit(x, x, k) : x; }
DI unsigned check_word(u32x4 a, u32x4 b, unsigned z, unsigned salt) {
  return ((a[0] + rot(a[1], 3) + rot(a[2], 5)) + (rot(a[3], 7) + rot(b[0], 9) + rot(b[1], 11))) + ((rot(b[2], 13) + rot(b[3], 15) + z) + salt);
}

#define DMA_QUAD(lds, g, v0, v1, v2, v3, POL)                                                   \
  asm volatile("s_mov_b32 m0, %0\n\ts_nop 0\n\t"                                                \
               "global_load_lds_dwordx4 %2, %1 offset:0 " POL "\n\t"                            \
               "global_load_lds_dwordx4 %3, %1 offset:1024 " POL "\n\t"                         \
               "global_load_lds_dwordx4 %4, %1 offset:2048 " POL "\n\t"                         \
               "global_load_lds_dwordx4 %5, %1 offset:3072 " POL                                \
               ::"s"(lds), "s"(g), "v"(v0), "v"(v1), "v"(v2), "v"(v3) : "memory")
#define ST128_DEV(g, voff, val, off)                                                            \
  asm volatile("global_store_dwordx4 %0, %1, %2 offset:%3 sc1\n\ts_nop 1" ::"v"(voff), "v"(val), "s"(g), "n"(off) : "memory")

DI void wait_ge(unsigned addr, unsigned need) {
  for (;;) {
    unsigned x;
    asm volatile("ds_read_b32 %0, %1\n\ts_waitcnt lgkmcnt(0)" : "=v"(x) : "v"(addr) : "memory");
    if ((unsigned)__builtin_amdgcn_readfirstlane(x) >= need) break;
    __builtin_amdgcn_s_sleep(1);
  }
}
DI void arrive(unsigned addr) {
  unsigned long long ex;
  asm volatile("s_mov_b64 %0, exec\n\ts_mov_b64 exec, 1\n\tds_add_u32 %1, %2\n\ts_mov_b64 exec, %0" : "=&s"(ex) : "v"(addr), "v"(1u) : "memory");
}
DI void post(unsigned addr, unsigned val) { asm volatile("ds_write_b32 %0, %1" ::"v"(addr), "v"(val) : "memory"); }

template <bool B> struct BC { static constexpr bool value = B; };

__global__ __launch_bounds__(576, 3) void rfa_kernel(const char* __restrict__ q, const char* __restrict__ k,
                                                     const char* __restrict__ v, const char* __restrict__ omega,
                                                     char* __restrict__ out, char* ws) {
  __shared__ __attribute__((aligned(16))) char smem[163840];
  unsigned long long hold0_;
  STAMP(0);
  const unsigned lds = (unsigned)(size_t)(LAS char*)smem;
  const int w = __builtin_amdgcn_readfirstlane(threadIdx.x >> 6);
  const unsigned lane = threadIdx.x & 63, r = lane & 31, h = lane >> 5;
  const int b = blockIdx.x, head = (b & 7) * 4 + (b >> 6), chunk = (b >> 3) & 7;
  const size_t row0 = (size_t)head * S + chunk * 256;
  const unsigned l16 = lane * 16;
  unsigned vs[4];
#pragma unroll
  for (int i = 0; i < 4; ++i) vs[i] = (lane >> 4) * 256 + (((lane & 15) ^ ((4 * i + (lane >> 4)) & 15)) << 4);
  const char* kk = k + row0 * 256;
  if (w == 8) {
#pragma unroll
    for (int pos = 0; pos < PRO; pos += 4) {
      if (pos < P_K) DMA_QUAD(lds + LDS_OM + pos * 1024, omega + pos * 1024, vs[0], vs[1], vs[2], vs[3], "");
      else DMA_QUAD(lds + LDS_A + (pos - P_K) * 1024, kk + (pos - P_K) * 1024, vs[0], vs[1], vs[2], vs[3], LD_POL);
    }
    post(lds + LDS_D + (lane & 7) * 4, 0u);
  }
  if (w == 7) {
    const unsigned pos = ((r >> 2) & 1) == h ? ((r >> 4) << 2) | (((r >> 3) & 1) << 1) | ((r >> 1) & 1) : 8u;
    const unsigned one = (r & 1) ? 0x3F800000u : 0x3F80u;
    u32x4 id0, id1;
#pragma unroll
    for (int j = 0; j < 4; ++j) { id0[j] = pos == j ? one : 0u; id1[j] = pos == 4 + j ? one : 0u; }
    lds_wr128(lds + LDS_ID + l16, id0);
    lds_wr128(lds + LDS_ID + 1024 + l16, id1);
  }
  asm volatile("s_waitcnt lgkmcnt(0)\n\ts_barrier" ::: "memory");

  if (w == 8) {
    const unsigned vv = (lane >> 4) * 256 + (((lane & 15) ^ ((lane >> 4) << 2)) << 4);
    const char* vsrc = v + row0 * 256;
    const char* qq = q + row0 * 256;
#pragma unroll
    for (int n = PRO - 16; n > WIN - 4; n -= 4) {
      WAIT_VM(n);
      post(lds + W_LANDED, PRO - n);
    }
#pragma unroll
    for (int pos = PRO; pos < P_END; pos += 4) {
      if (pos == P_V + 48) wait_ge(lds + W_OM, 8);
      if (pos == P_Q + 24) wait_ge(lds + W_AF, 8);
      if (pos < P_V) DMA_QUAD(lds + LDS_A + (pos - P_K) * 1024, kk + (pos - P_K) * 1024, vs[0], vs[1], vs[2], vs[3], LD_POL);
      else if (pos < P_Q) DMA_QUAD(lds + LDS_B + (pos - P_V) * 1024, vsrc + (pos - P_V) * 1024, vv, vv, vv, vv, LD_POL);
      else {
        const int o = (pos - P_Q) * 1024;
        DMA_QUAD(lds + (o < 24576 ? LDS_C + o : LDS_A + o - 24576), qq + o, vs[0], vs[1], vs[2], vs[3], LD_POL);
      }
      WAIT_VM(WIN);
      post(lds + W_LANDED, pos + 4 - WIN);
      if (pos + 4 == P_V) STAMP(1);
      if (pos + 4 == P_Q) STAMP(2);
    }
    STAMP(3);
#pragma unroll
    for (int n = WIN - 4; n >= 0; n -= 4) {
      WAIT_VM(n);
      post(lds + W_LANDED, P_END - n);
    }
    STAMP(4);
    return;
  }

  const int mt = (w >> 1) & 1, dt = (w & 1) | ((w >> 2) << 1);
  const bool zwave = dt == 0;
  char* epw = ws + WS_EP + head * 128;
  char* auxbuf = ws + WS_AUX + (size_t)(head * 8 + w) * 4096;
  char* kvbuf = ws + WS_KV + (size_t)(head * 8 + w) * 16384;
  __builtin_amdgcn_s_setprio(1);
  unsigned ep;
  asm volatile("global_load_dword %0, %1, %2 sc1" : "=v"(ep) : "v"(0u), "s"(epw) : "memory");
  STAMP(1);

  const unsigned xr = r * 256 + ((h ^ (r & 15)) << 4);
  u32x4 om[2][8];
  wait_ge(lds + W_LANDED, P_K);
#pragma unroll
  for (int i = 0; i < 2; ++i)
#pragma unroll
    for (int t = 0; t < 8; ++t) om[i][t] = lds_rd128(((lds + LDS_OM + xr) ^ (t << 5)) + i * 8192);
  arrive(lds + W_OM);

  const float scale = 0.29730177875068026f;
  const float c1 = scale * 1.4426950408889634f;
  const float c2 = -0.5f * scale * scale * 1.4426950408889634f;

  {
    const unsigned slotA = lds + LDS_A + w * 8192;
    wait_ge(lds + W_LANDED, P_K + 8 * (w + 1));
    u32x4 kf[8];
    unsigned ka = slotA + xr;
    asm volatile("" : "+v"(ka));
#pragma unroll
    for (int t = 0; t < 8; ++t) kf[t] = lds_rd128(ka ^ (t << 5));
    SCHED_FENCE();
    f32x16 acc[2] = {};
    float ss[4] = {};
#pragma unroll
    for (int t = 0; t < 8; ++t)
#pragma unroll
      for (int i = 0; i < 4; ++i) ss[i] = dot2(kf[t][i], kf[t][i], ss[i]);
#pragma unroll
    for (int t = 0; t < 8; ++t) acc[0] = MFMA(as_frag(kf[t]), as_frag(om[0][t]), acc[0]);
    const float bias = add_halves((ss[0] + ss[1]) + (ss[2] + ss[3])) * c2 - 3.0f;
    *(LAS float*)(size_t)(slotA + 4096 + r * 4) = bias;
    f32x4 bz[4];
#pragma unroll
    for (int g = 0; g < 4; ++g) bz[g] = *(const LAS f32x4*)(size_t)(slotA + 4096 + (8 * g + 4 * h) * 4);
#pragma unroll
    for (int t = 0; t < 8; ++t) acc[1] = MFMA(as_frag(kf[t]), as_frag(om[1][t]), acc[1]);
#pragma unroll
    for (int i = 0; i < 2; ++i)
#pragma unroll
      for (int s = 0; s < 2; ++s) {
        u32x4 f;
#pragma unroll
        for (int j = 0; j < 4; ++j) {
          const int e = 8 * s + 2 * j;
          const float p0 = __builtin_amdgcn_exp2f(acc[i][e] * c1 + bz[e >> 2][e & 3]) + 1e-6f;
          const float p1 = __builtin_amdgcn_exp2f(acc[i][e + 1] * c1 + bz[(e + 1) >> 2][(e + 1) & 3]) + 1e-6f;
          f[j] = pk_bf16(p0, p1);
        }
        lds_wr128(slotA + (i * 2 + s) * 1024 + l16, f);
      }
  }
  arrive(lds + W_PHI);
  asm volatile("s_waitcnt vmcnt(0)" : "+v"(ep)::"memory");
  const unsigned tag = __builtin_amdgcn_readfirstlane(ep) + 1;
  const unsigned salt = tag + lane * 0x9E3779B1u;
  wait_ge(lds + W_PHI, 8);
  STAMP(4);

  f32x16 kv = {};
  float zown = 0.f;
  {
    const unsigned ab = lds + LDS_A + mt * 2048 + l16;
    u32x4 af[16];
#pragma unroll
    for (int ks = 0; ks < 10; ++ks) af[ks] = lds_rd128(ab + (ks >> 1) * 8192 + (ks & 1) * 1024);
    arrive(lds + W_AF);
    float zs[4] = {};
    const unsigned qq = (lane & 15) >> 2, p = lane & 3, blk = (lane >> 4) & 1;
    const unsigned trb = lds + LDS_B + (4 * h + qq) * 256 + (((4 * dt + 2 * blk + (p >> 1)) ^ (qq << 2)) << 4) + 8 * (p & 1);
#pragma unroll
    for (int c = 0; c < 4; ++c) {
      if (c == 1 || c == 2) {
#pragma unroll
        for (int ks = (c == 1 ? 10 : 12); ks < (c == 1 ? 12 : 16); ++ks) af[ks] = lds_rd128(ab + (ks >> 1) * 8192 + (ks & 1) * 1024);
      }
      if (zwave && c < 3) {
#pragma unroll
        for (int ks = (c == 0 ? 0 : 4 * c + 4); ks < 4 * c + 8; ++ks)
#pragma unroll
          for (int i = 0; i < 4; ++i) zs[i] = dot2(af[ks][i], 0x3F803F80u, zs[i]);
        if (c == 2) zown = add_halves((zs[0] + zs[1]) + (zs[2] + zs[3]));
      }
      SCHED_FENCE();
      wait_ge(lds + W_LANDED, P_V + 16 * (c + 1));
      if (c == 3) STAMP(12);
      s16x4 vf[8];
#pragma unroll
      for (int u = 0; u < 8; ++u) vf[u] = __builtin_amdgcn_ds_read_tr16_b64_v4i16((LAS s16x4*)(size_t)(trb + (64 * c + 8 * u) * 256));
      SCHED_FENCE();
#pragma unroll
      for (int u = 0; u < 4; ++u)
        kv = MFMA(as_frag(af[4 * c + u]), __builtin_bit_cast(bf16x8, __builtin_shufflevector(vf[2 * u], vf[2 * u + 1], 0, 1, 2, 3, 4, 5, 6, 7)), kv);
    }
    arrive(lds + W_KV);
  }
  {
    u32x4 d0, d1;
    pack_tile(kv, d0, d1);
    ST128_DEV(kvbuf + chunk * 2048, l16, d0, 0);
    ST128_DEV(kvbuf + chunk * 2048, l16, d1, 1024);
    const unsigned zb = __builtin_bit_cast(unsigned, zown);
    const u32x2 ax = {check_word(d0, d1, zb, salt), zb};
    if (zwave) asm volatile("global_store_dwordx2 %0, %1, %2 sc1" ::"v"(lane * 8), "v"(ax), "s"(auxbuf + chunk * 512) : "memory");
    else asm volatile("global_store_dword %0, %1, %2 sc1" ::"v"(lane * 4), "v"(ax[0]), "s"(auxbuf + chunk * 512) : "memory");
  }
  STAMP(5);

  if (w >= 4) __builtin_amdgcn_s_setprio(2);
  const unsigned slotQ = w < 3 ? lds + LDS_C + w * 8192 : lds + LDS_A + (w - 3) * 8192;
  u32x4 xq[4];
  {
    wait_ge(lds + W_LANDED, P_Q + 8 * (w + 1));
    STAMP(7);
    u32x4 qf[8];
    unsigned qa = slotQ + xr;
    asm volatile("" : "+v"(qa));
#pragma unroll
    for (int t = 0; t < 8; ++t) qf[t] = lds_rd128(qa ^ (t << 5));
    SCHED_FENCE();
    f32x16 acc[2] = {};
    float ss[4] = {};
#pragma unroll
    for (int t = 0; t < 8; ++t)
#pragma unroll
      for (int i = 0; i < 4; ++i) ss[i] = dot2(qf[t][i], qf[t][i], ss[i]);
#pragma unroll
    for (int t = 0; t < 8; ++t) acc[0] = MFMA(as_frag(om[0][t]), as_frag(qf[t]), acc[0]);
    const float bias = add_halves((ss[0] + ss[1]) + (ss[2] + ss[3])) * c2 - 3.0f;
#pragma unroll
    for (int t = 0; t < 8; ++t) acc[1] = MFMA(as_frag(om[1][t]), as_frag(qf[t]), acc[1]);
#pragma unroll
    for (int u = 0; u < 4; ++u)
#pragma unroll
      for (int j = 0; j < 4; ++j) {
        const int e = 8 * (u & 1) + 2 * j;
        xq[u][j] = pk_bf16(__builtin_amdgcn_exp2f(acc[u >> 1][e] * c1 + bias) + 1e-6f, __builtin_amdgcn_exp2f(acc[u >> 1][e + 1] * c1 + bias) + 1e-6f);
      }
    asm volatile("" : "+v"(xq[0]), "+v"(xq[1]), "+v"(xq[2]), "+v"(xq[3]));
    SCHED_FENCE();
  }
  unsigned wb = slotQ + r * 256 + 8 * (h ^ ((r >> 3) & 1)), wx = (r & 7) << 4;
  char* dst = out + (row0 + w * 32) * 256;
  unsigned so = (lane >> 3) * 256 + (lane & 7) * 16;
  unsigned rb = slotQ + (lane >> 3) * 256 + (((lane & 7) ^ (lane >> 3)) << 4);
  unsigned kb = lds + LDS_B + l16, zb = lds + LDS_Z + h * 16;
  asm volatile("" : "+v"(wb), "+v"(wx), "+v"(so), "+v"(rb), "+v"(kb), "+v"(zb), "+s"(dst));

  __builtin_amdgcn_s_setprio(1);
  wait_ge(lds + W_KV, 8);
  wait_ge(lds + W_LANDED, P_END - GATE);
  STAMP(6);
  auto gather = [&](auto with_z) {
    constexpr bool Z = decltype(with_z)::value;
    u32x4 pa[7], pb[7];
    unsigned pc[7];
    u32x2 px[7];
#define REC_LOAD(i, TAIL, C)                                                                                                          \
  do { const int c_ = (chunk + 1 + (i)) & 7;                                                                                          \
       if (Z) asm volatile("global_load_dwordx4 %0, %3, %5 sc1\n\tglobal_load_dwordx4 %1, %3, %5 offset:1024 sc1\n\t"                 \
                           "global_load_dwordx2 %2, %4, %6 sc1" TAIL : C(pa[i]), C(pb[i]), C(px[i])                                   \
                           : "v"(l16), "v"(lane * 8), "s"(kvbuf + c_ * 2048), "s"(auxbuf + c_ * 512) : "memory");                     \
       else asm volatile("global_load_dwordx4 %0, %3, %5 sc1\n\tglobal_load_dwordx4 %1, %3, %5 offset:1024 sc1\n\t"                   \
                         "global_load_dword %2, %4, %6 sc1" TAIL : C(pa[i]), C(pb[i]), C(pc[i])                                       \
                         : "v"(l16), "v"(lane * 4), "s"(kvbuf + c_ * 2048), "s"(auxbuf + c_ * 512) : "memory"); } while (0)
#define REC_BAD(i) (__builtin_amdgcn_ballot_w64(check_word(pa[i], pb[i], Z ? px[i][1] : 0u, salt) != (Z ? px[i][0] : pc[i])) != 0)
#pragma unroll
    for (int i = 0; i < 7; ++i) REC_LOAD(i, "", "=&v");
    if (w >= 4) __builtin_amdgcn_s_setprio(2);
    STAMP(14);
    const u32x4 id0 = lds_rd128(lds + LDS_ID + l16), id1 = lds_rd128(lds + LDS_ID + 1024 + l16);
    unsigned spin = 0;
#pragma unroll
    for (int i = 0; i < 7; ++i) {
      if (Z) asm volatile("s_waitcnt vmcnt(%3)" : "+v"(pa[i]), "+v"(pb[i]), "+v"(px[i]) : "n"(18 - 3 * i) : "memory");
      else asm volatile("s_waitcnt vmcnt(%3)" : "+v"(pa[i]), "+v"(pb[i]), "+v"(pc[i]) : "n"(18 - 3 * i) : "memory");
      if (__builtin_expect(REC_BAD(i), 0)) {
        do {
          COUNT_RETRY();
          __builtin_amdgcn_s_sleep(2);
          REC_LOAD(i, "\n\ts_waitcnt vmcnt(0)", "+v");
        } while (REC_BAD(i) && ++spin < SPIN_CAP);
      }
      kv = MFMA(as_frag(id0), as_frag(pa[i]), kv);
      kv = MFMA(as_frag(id1), as_frag(pb[i]), kv);
      if (Z) { const unsigned zu = px[i][1]; zown += __builtin_bit_cast(float, zu); }
    }
    STAMP(8);
    if (Z && lane < 32)
      *(LAS unsigned short*)(size_t)(lds + LDS_Z + mt * 64 + (((r >> 4) * 2 + ((r >> 2) & 1)) * 8 + ((r >> 3) & 1) * 4 + (r & 3)) * 2) = (unsigned short)pk_bf16(zown, 0.f);
    u32x4 d0, d1;
    pack_tile(kv, d0, d1);
    lds_wr128(kb + (mt * 4 + dt) * 2048, d0);
    lds_wr128(kb + (mt * 4 + dt) * 2048 + 1024, d1);
    arrive(lds + W_H + (w >> 2) * 4);
  };
  if (zwave) gather(BC<true>{}); else gather(BC<false>{});
  STAMP(9);

  {
    float inv;
#pragma unroll
    for (int half = 0; half < 2; ++half) {
      wait_ge(lds + W_H + half * 4, 4);
      if (half) STAMP(3); else STAMP(10);
      u32x4 zf[4], kf2[2][4];
      if (half == 0) {
#pragma unroll
        for (int u = 0; u < 4; ++u) zf[u] = lds_rd128(zb + u * 32);
      }
#pragma unroll
      for (int d = 0; d < 2; ++d) {
        SCHED_FENCE();
#pragma unroll
        for (int u = 0; u < 4; ++u) kf2[d][u] = lds_rd128(kb + ((u >> 1) * 4 + 2 * half + d) * 2048 + (u & 1) * 1024);
      }
      SCHED_FENCE();
      f32x16 den = {}, o[2] = {};
      if (half == 0) {
#pragma unroll
        for (int u = 0; u < 4; ++u) den = MFMA(as_frag(zf[u]), as_frag(xq[u]), den);
      }
#pragma unroll
      for (int d = 0; d < 2; ++d)
#pragma unroll
        for (int u = 0; u < 4; ++u) o[d] = MFMA(as_frag(kf2[d][u]), as_frag(xq[u]), o[d]);
      if (half == 0) inv = __builtin_amdgcn_rcpf(den[0] + 1e-6f);
#pragma unroll
      for (int d = 0; d < 2; ++d)
#pragma unroll
        for (int g = 0; g < 4; ++g) {
          const u32x2 pk = {pk_bf16(o[d][4 * g] * inv, o[d][4 * g + 1] * inv), pk_bf16(o[d][4 * g + 2] * inv, o[d][4 * g + 3] * inv)};
          lds_wr64(wb + (((8 * half + 4 * d + g) << 4) ^ wx), pk);
        }
      u32x4 x[4];
#pragma unroll
      for (int i = 0; i < 4; ++i) x[i] = lds_rd128(rb + i * 2048 + half * 128);
#pragma unroll
      for (int i = 0; i < 4; ++i) {
        if (i & 1) x[i] = (u32x4){x[i][2], x[i][3], x[i][0], x[i][1]};
        ST128_DEV(dst, so + (i >> 1) * 4096, x[i], (i & 1) * 2048 + half * 128);
      }
      if (half == 0) STAMP(2);
    }
  }
  if (chunk == 0 && w == 0 && lane == 0)
    asm volatile("global_store_dword %0, %1, %2 sc1" ::"v"(0u), "v"(tag), "s"(epw) : "memory");
  STAMP(11);
}

extern "C" void kernel_launch(void* const* d_in, const int* in_sizes, int n_in, void* d_out, int out_size, void* d_ws,
                              size_t ws_size, hipStream_t stream) {
  rfa_kernel<<<256, 576, 0, stream>>>((const char*)d_in[0], (const char*)d_in[1], (const char*)d_in[2], (const char*)d_in[3],
                                      (char*)d_out, (char*)d_ws);
}
```

```cpp
#include <hip/hip_runtime.h>
#include <hip/hip_bf16.h>
#include <stdint.h>

typedef __bf16 bf16x8 __attribute__((ext_vector_type(8)));
typedef __bf16 bf16x2 __attribute__((ext_vector_type(2)));
typedef short s16x4 __attribute__((ext_vector_type(4)));
typedef float f32x16 __attribute__((ext_vector_type(16)));
typedef float f32x4 __attribute__((ext_vector_type(4)));
typedef unsigned u32x4 __attribute__((ext_vector_type(4)));
typedef unsigned u32x2 __attribute__((ext_vector_type(2)));
#define LAS __attribute__((address_space(3)))
#define DI __device__ __forceinline__

#define LD_POL "nt"
#define WIN 24
#define PRO 40
#define GATE 16
#define CODE_LINES 180
#define ROT 1

constexpr int S = 2048;
constexpr int LDS_A = 0;
constexpr int LDS_B = 65536;
constexpr int LDS_OM = LDS_B + 49152;
constexpr int LDS_C = 131072;
constexpr int LDS_D = 155648;
constexpr int W_LANDED = LDS_D, W_OM = LDS_D + 4, W_PHI = LDS_D + 8, W_AF = LDS_D + 12, W_KV = LDS_D + 16, W_H = LDS_D + 20;
constexpr int LDS_Z = LDS_D + 128, LDS_ID = LDS_D + 256;
constexpr int P_K = 16, P_V = 80, P_Q = 144, P_END = 208;
constexpr size_t WS_EP = 0;
constexpr size_t WS_AUX = 65536;
constexpr size_t WS_KV = WS_AUX + 1048576;
constexpr unsigned SPIN_CAP = 1u << 18;

#define STAMP(i)
#define COUNT_RETRY()
#define MFMA(a, b, c) __builtin_amdgcn_mfma_f32_32x32x16_bf16((a), (b), (c), 0, 0, 0)
#define WAIT_VM(n) asm volatile("s_waitcnt vmcnt(%0)" ::"n"(n) : "memory")
#define SCHED_FENCE() __builtin_amdgcn_sched_barrier(0)

DI unsigned pk_bf16(float lo, float hi) { bf16x2 p; p[0] = (__bf16)lo; p[1] = (__bf16)hi; return __builtin_bit_cast(unsigned, p); }
DI bf16x8 as_frag(u32x4 v) { return __builtin_bit_cast(bf16x8, v); }
DI u32x4 lds_rd128(unsigned a) { return *(const LAS u32x4*)(size_t)a; }
DI void lds_wr128(unsigned a, u32x4 v) { *(LAS u32x4*)(size_t)a = v; }
DI void lds_wr64(unsigned a, u32x2 v) { *(LAS u32x2*)(size_t)a = v; }
DI float dot2(unsigned a, unsigned b, float c) { return __builtin_amdgcn_fdot2_f32_bf16(__builtin_bit_cast(bf16x2, a), __builtin_bit_cast(bf16x2, b), c, false); }
DI float add_halves(float x) {
  const unsigned u = __builtin_bit_cast(unsigned, x);
  const auto r = __builtin_amdgcn_permlane32_swap(u, u, false, false);
  return __builtin_bit_cast(float, (unsigned)r[0]) + __builtin_bit_cast(float, (unsigned)r[1]);
}
DI void pack_tile(const f32x16& t, u32x4& d0, u32x4& d1) {
#pragma unroll
  for (int j = 0; j < 4; ++j) { d0[j] = pk_bf16(t[2 * j], t[2 * j + 1]); d1[j] = pk_bf16(t[8 + 2 * j], t[8 + 2 * j + 1]); }
}
DI unsigned rot(unsigned x, int k) { return ROT ? __builtin_amdgcn_alignbit(x, x, k) : x; }
DI unsigned check_word(u32x4 a, u32x4 b, unsigned z, unsigned salt) {
  return ((a[0] + rot(a[1], 3) + rot(a[2], 5)) + (rot(a[3], 7) + rot(b[0], 9) + rot(b[1], 11))) + ((rot(b[2], 13) + rot(b[3], 15) + z) + salt);
}

#define DMA_QUAD(lds, g, v0, v1, v2, v3, POL)                                                   \
  asm volatile("s_mov_b32 m0, %0\n\ts_nop 0\n\t"                                                \
               "global_load_lds_dwordx4 %2, %1 offset:0 " POL "\n\t"                            \
               "global_load_lds_dwordx4 %3, %1 offset:1024 " POL "\n\t"                         \
               "global_load_lds_dwordx4 %4, %1 offset:2048 " POL "\n\t"                         \
               "global_load_lds_dwordx4 %5, %1 offset:3072 " POL                                \
               ::"s"(lds), "s"(g), "v"(v0), "v"(v1), "v"(v2), "v"(v3) : "memory")
#define ST128_DEV(g, voff, val, off)                                                            \
  asm volatile("global_store_dwordx4 %0, %1, %2 offset:%3 sc1\n\ts_nop 1" ::"v"(voff), "v"(val), "s"(g), "n"(off) : "memory")

DI void wait_ge(unsigned addr, unsigned need) {
  for (;;) {
    unsigned x;
    asm volatile("ds_read_b32 %0, %1\n\ts_waitcnt lgkmcnt(0)" : "=v"(x) : "v"(addr) : "memory");
    if ((unsigned)__builtin_amdgcn_readfirstlane(x) >= need) break;
    __builtin_amdgcn_s_sleep(1);
  }
}
DI void arrive(unsigned addr) {
  unsigned long long ex;
  asm volatile("s_mov_b64 %0, exec\n\ts_mov_b64 exec, 1\n\tds_add_u32 %1, %2\n\ts_mov_b64 exec, %0" : "=&s"(ex) : "v"(addr), "v"(1u) : "memory");
}
DI void post(unsigned addr, unsigned val) { asm volatile("ds_write_b32 %0, %1" ::"v"(addr), "v"(val) : "memory"); }

template <bool B> struct BC { static constexpr bool value = B; };

__global__ __launch_bounds__(576, 3) void rfa_kernel(const char* __restrict__ q, const char* __restrict__ k,
                                                     const char* __restrict__ v, const char* __restrict__ omega,
                                                     char* __restrict__ out, char* ws) {
  __shared__ __attribute__((aligned(16))) char smem[163840];
  STAMP(0);
  unsigned long long pc0;
  asm volatile("s_getpc_b64 %0" : "=s"(pc0));
  const unsigned lds = (unsigned)(size_t)(LAS char*)smem;
  const int w = __builtin_amdgcn_readfirstlane(threadIdx.x >> 6);
  const unsigned lane = threadIdx.x & 63, r = lane & 31, h = lane >> 5;
  const int b = blockIdx.x, head = (b & 7) * 4 + (b >> 6), chunk = (b >> 3) & 7;
  const size_t row0 = (size_t)head * S + chunk * 256;
  const unsigned l16 = lane * 16;
  unsigned vs[4];
#pragma unroll
  for (int i = 0; i < 4; ++i) vs[i] = (lane >> 4) * 256 + (((lane & 15) ^ ((4 * i + (lane >> 4)) & 15)) << 4);
  const char* kk = k + row0 * 256;
  if (w == 8) {
#pragma unroll
    for (int pos = 0; pos < PRO; pos += 4) {
      if (pos < P_K) DMA_QUAD(lds + LDS_OM + pos * 1024, omega + pos * 1024, vs[0], vs[1], vs[2], vs[3], "");
      else DMA_QUAD(lds + LDS_A + (pos - P_K) * 1024, kk + (pos - P_K) * 1024, vs[0], vs[1], vs[2], vs[3], LD_POL);
    }
    post(lds + LDS_D + (lane & 7) * 4, 0u);
  }
  if (w == 7) {
    const unsigned pos = ((r >> 2) & 1) == h ? ((r >> 4) << 2) | (((r >> 3) & 1) << 1) | ((r >> 1) & 1) : 8u;
    const unsigned one = (r & 1) ? 0x3F800000u : 0x3F80u;
    u32x4 id0, id1;
#pragma unroll
    for (int j = 0; j < 4; ++j) { id0[j] = pos == j ? one : 0u; id1[j] = pos == 4 + j ? one : 0u; }
    lds_wr128(lds + LDS_ID + l16, id0);
    lds_wr128(lds + LDS_ID + 1024 + l16, id1);
  }
  asm volatile("s_waitcnt lgkmcnt(0)\n\ts_barrier" ::: "memory");

  if (w == 8) {
    const unsigned vv = (lane >> 4) * 256 + (((lane & 15) ^ ((lane >> 4) << 2)) << 4);
    const char* vsrc = v + row0 * 256;
    const char* qq = q + row0 * 256;
#pragma unroll
    for (int n = PRO - 16; n > WIN - 4; n -= 4) {
      WAIT_VM(n);
      post(lds + W_LANDED, PRO - n);
    }
#pragma unroll
    for (int pos = PRO; pos < P_END; pos += 4) {
      if (pos == P_V + 48) wait_ge(lds + W_OM, 8);
      if (pos == P_Q + 24) wait_ge(lds + W_AF, 8);
      if (pos < P_V) DMA_QUAD(lds + LDS_A + (pos - P_K) * 1024, kk + (pos - P_K) * 1024, vs[0], vs[1], vs[2], vs[3], LD_POL);
      else if (pos < P_Q) DMA_QUAD(lds + LDS_B + (pos - P_V) * 1024, vsrc + (pos - P_V) * 1024, vv, vv, vv, vv, LD_POL);
      else {
        const int o = (pos - P_Q) * 1024;
        DMA_QUAD(lds + (o < 24576 ? LDS_C + o : LDS_A + o - 24576), qq + o, vs[0], vs[1], vs[2], vs[3], LD_POL);
      }
      WAIT_VM(WIN);
      post(lds + W_LANDED, pos + 4 - WIN);
      if (pos + 4 == P_V) STAMP(1);
      if (pos + 4 == P_Q) STAMP(2);
    }
    STAMP(3);
#pragma unroll
    for (int n = WIN - 4; n >= 0; n -= 4) {
      WAIT_VM(n);
      post(lds + W_LANDED, P_END - n);
    }
    STAMP(4);
    return;
  }

  const int mt = (w >> 1) & 1, dt = (w & 1) | ((w >> 2) << 1);
  const bool zwave = dt == 0;
  char* epw = ws + WS_EP + head * 128;
  char* auxbuf = ws + WS_AUX + (size_t)(head * 8 + w) * 4096;
  char* kvbuf = ws + WS_KV + (size_t)(head * 8 + w) * 16384;
  __builtin_amdgcn_s_setprio(1);
  unsigned ep;
  asm volatile("global_load_dword %0, %1, %2 sc1" : "=v"(ep) : "v"(0u), "s"(epw) : "memory");
  unsigned warm, warm2;
  asm volatile("s_load_dword %0, %2, 0x0\n\ts_load_dword %1, %2, 0x40" : "=&s"(warm), "=&s"(warm2) : "s"((pc0 & ~255ull) + min((b >> 3) * 8 + w, CODE_LINES - 1) * 128) : "memory");
  STAMP(1);

  const unsigned xr = r * 256 + ((h ^ (r & 15)) << 4);
  u32x4 om[2][8];
  wait_ge(lds + W_LANDED, P_K);
  asm volatile("" ::"s"(warm), "s"(warm2));
#pragma unroll
  for (int i = 0; i < 2; ++i)
#pragma unroll
    for (int t = 0; t < 8; ++t) om[i][t] = lds_rd128(((lds + LDS_OM + xr) ^ (t << 5)) + i * 8192);
  arrive(lds + W_OM);

  const float scale = 0.29730177875068026f;
  const float c1 = scale * 1.4426950408889634f;
  const float c2 = -0.5f * scale * scale * 1.4426950408889634f;

  {
    const unsigned slotA = lds + LDS_A + w * 8192;
    wait_ge(lds + W_LANDED, P_K + 8 * (w + 1));
    u32x4 kf[8];
    unsigned ka = slotA + xr;
    asm volatile("" : "+v"(ka));
#pragma unroll
    for (int t = 0; t < 8; ++t) kf[t] = lds_rd128(ka ^ (t << 5));
    SCHED_FENCE();
    f32x16 acc[2] = {};
    float ss[4] = {};
#pragma unroll
    for (int t = 0; t < 8; ++t)
#pragma unroll
      for (int i = 0; i < 4; ++i) ss[i] = dot2(kf[t][i], kf[t][i], ss[i]);
#pragma unroll
    for (int t = 0; t < 8; ++t) acc[0] = MFMA(as_frag(kf[t]), as_frag(om[0][t]), acc[0]);
    const float bias = add_halves((ss[0] + ss[1]) + (ss[2] + ss[3])) * c2 - 3.0f;
    *(LAS float*)(size_t)(slotA + 4096 + r * 4) = bias;
    f32x4 bz[4];
#pragma unroll
    for (int g = 0; g < 4; ++g) bz[g] = *(const LAS f32x4*)(size_t)(slotA + 4096 + (8 * g + 4 * h) * 4);
#pragma unroll
    for (int t = 0; t < 8; ++t) acc[1] = MFMA(as_frag(kf[t]), as_frag(om[1][t]), acc[1]);
#pragma unroll
    for (int i = 0; i < 2; ++i)
#pragma unroll
      for (int s = 0; s < 2; ++s) {
        u32x4 f;
#pragma unroll
        for (int j = 0; j < 4; ++j) {
          const int e = 8 * s + 2 * j;
          const float p0 = __builtin_amdgcn_exp2f(acc[i][e] * c1 + bz[e >> 2][e & 3]) + 1e-6f;
          const float p1 = __builtin_amdgcn_exp2f(acc[i][e + 1] * c1 + bz[(e + 1) >> 2][(e + 1) & 3]) + 1e-6f;
          f[j] = pk_bf16(p0, p1);
        }
        lds_wr128(slotA + (i * 2 + s) * 1024 + l16, f);
      }
  }
  arrive(lds + W_PHI);
  asm volatile("s_waitcnt vmcnt(0)" : "+v"(ep)::"memory");
  unsigned touch;
  asm volatile("global_load_dword %0, %1, %2 nt" : "=v"(touch) : "v"(0u), "s"(out + (row0 + w * 32) * 256) : "memory");
  const unsigned tag = __builtin_amdgcn_readfirstlane(ep) + 1;
  const unsigned salt = tag + lane * 0x9E3779B1u;
  wait_ge(lds + W_PHI, 8);
  STAMP(4);

  f32x16 kv = {};
  float zown = 0.f;
  {
    const unsigned ab = lds + LDS_A + mt * 2048 + l16;
    u32x4 af[16];
#pragma unroll
    for (int ks = 0; ks < 10; ++ks) af[ks] = lds_rd128(ab + (ks >> 1) * 8192 + (ks & 1) * 1024);
    arrive(lds + W_AF);
    float zs[4] = {};
    const unsigned qq = (lane & 15) >> 2, p = lane & 3, blk = (lane >> 4) & 1;
    const unsigned trb = lds + LDS_B + (4 * h + qq) * 256 + (((4 * dt + 2 * blk + (p >> 1)) ^ (qq << 2)) << 4) + 8 * (p & 1);
#pragma unroll
    for (int c = 0; c < 4; ++c) {
      if (c == 1 || c == 2) {
#pragma unroll
        for (int ks = (c == 1 ? 10 : 12); ks < (c == 1 ? 12 : 16); ++ks) af[ks] = lds_rd128(ab + (ks >> 1) * 8192 + (ks & 1) * 1024);
      }
      if (zwave && c < 3) {
#pragma unroll
        for (int ks = (c == 0 ? 0 : 4 * c + 4); ks < 4 * c + 8; ++ks)
#pragma unroll
          for (int i = 0; i < 4; ++i) zs[i] = dot2(af[ks][i], 0x3F803F80u, zs[i]);
        if (c == 2) zown = add_halves((zs[0] + zs[1]) + (zs[2] + zs[3]));
      }
      SCHED_FENCE();
      wait_ge(lds + W_LANDED, P_V + 16 * (c + 1));
      if (c == 3) STAMP(12);
      s16x4 vf[8];
#pragma unroll
      for (int u = 0; u < 8; ++u) vf[u] = __builtin_amdgcn_ds_read_tr16_b64_v4i16((LAS s16x4*)(size_t)(trb + (64 * c + 8 * u) * 256));
      SCHED_FENCE();
#pragma unroll
      for (int u = 0; u < 4; ++u)
        kv = MFMA(as_frag(af[4 * c + u]), __builtin_bit_cast(bf16x8, __builtin_shufflevector(vf[2 * u], vf[2 * u + 1], 0, 1, 2, 3, 4, 5, 6, 7)), kv);
    }
    arrive(lds + W_KV);
    asm volatile("s_waitcnt vmcnt(0)" : "+v"(touch)::"memory");
  }
  {
    u32x4 d0, d1;
    pack_tile(kv, d0, d1);
    ST128_DEV(kvbuf + chunk * 2048, l16, d0, 0);
    ST128_DEV(kvbuf + chunk * 2048, l16, d1, 1024);
    const unsigned zb = __builtin_bit_cast(unsigned, zown);
    const u32x2 ax = {check_word(d0, d1, zb, salt), zb};
    if (zwave) asm volatile("global_store_dwordx2 %0, %1, %2 sc1" ::"v"(lane * 8), "v"(ax), "s"(auxbuf + chunk * 512) : "memory");
    else asm volatile("global_store_dword %0, %1, %2 sc1" ::"v"(lane * 4), "v"(ax[0]), "s"(auxbuf + chunk * 512) : "memory");
  }
  STAMP(5);

  if (w >= 4) __builtin_amdgcn_s_setprio(2);
  const unsigned slotQ = w < 3 ? lds + LDS_C + w * 8192 : lds + LDS_A + (w - 3) * 8192;
  u32x4 xq[4];
  {
    wait_ge(lds + W_LANDED, P_Q + 8 * (w + 1));
    STAMP(7);
    u32x4 qf[8];
    unsigned qa = slotQ + xr;
    asm volatile("" : "+v"(qa));
#pragma unroll
    for (int t = 0; t < 8; ++t) qf[t] = lds_rd128(qa ^ (t << 5));
    SCHED_FENCE();
    f32x16 acc[2] = {};
    float ss[4] = {};
#pragma unroll
    for (int t = 0; t < 8; ++t)
#pragma unroll
      for (int i = 0; i < 4; ++i) ss[i] = dot2(qf[t][i], qf[t][i], ss[i]);
#pragma unroll
    for (int t = 0; t < 8; ++t) acc[0] = MFMA(as_frag(om[0][t]), as_frag(qf[t]), acc[0]);
    const float bias = add_halves((ss[0] + ss[1]) + (ss[2] + ss[3])) * c2 - 3.0f;
#pragma unroll
    for (int t = 0; t < 8; ++t) acc[1] = MFMA(as_frag(om[1][t]), as_frag(qf[t]), acc[1]);
#pragma unroll
    for (int u = 0; u < 4; ++u)
#pragma unroll
      for (int j = 0; j < 4; ++j) {
        const int e = 8 * (u & 1) + 2 * j;
        xq[u][j] = pk_bf16(__builtin_amdgcn_exp2f(acc[u >> 1][e] * c1 + bias) + 1e-6f, __builtin_amdgcn_exp2f(acc[u >> 1][e + 1] * c1 + bias) + 1e-6f);
      }
    asm volatile("" : "+v"(xq[0]), "+v"(xq[1]), "+v"(xq[2]), "+v"(xq[3]));
    SCHED_FENCE();
  }
  unsigned wb = slotQ + r * 256 + 8 * (h ^ ((r >> 3) & 1)), wx = (r & 7) << 4;
  char* dst = out + (row0 + w * 32) * 256;
  unsigned so = (lane >> 3) * 256 + (lane & 7) * 16;
  unsigned rb = slotQ + (lane >> 3) * 256 + (((lane & 7) ^ (lane >> 3)) << 4);
  unsigned kb = lds + LDS_B + l16, zb = lds + LDS_Z + h * 16;
  asm volatile("" : "+v"(wb), "+v"(wx), "+v"(so), "+v"(rb), "+v"(kb), "+v"(zb), "+s"(dst));

  __builtin_amdgcn_s_setprio(1);
  wait_ge(lds + W_KV, 8);
  wait_ge(lds + W_LANDED, P_END - GATE);
  STAMP(6);
  auto gather = [&](auto with_z) {
    constexpr bool Z = decltype(with_z)::value;
    u32x4 pa[7], pb[7];
    unsigned pc[7];
    u32x2 px[7];
#define REC_LOAD(i, TAIL, C)                                                                                                          \
  do { const int c_ = (chunk + 1 + (i)) & 7;                                                                                          \
       if (Z) asm volatile("global_load_dwordx4 %0, %3, %5 sc1\n\tglobal_load_dwordx4 %1, %3, %5 offset:1024 sc1\n\t"                 \
                           "global_load_dwordx2 %2, %4, %6 sc1" TAIL : C(pa[i]), C(pb[i]), C(px[i])                                   \
                           : "v"(l16), "v"(lane * 8), "s"(kvbuf + c_ * 2048), "s"(auxbuf + c_ * 512) : "memory");                     \
       else asm volatile("global_load_dwordx4 %0, %3, %5 sc1\n\tglobal_load_dwordx4 %1, %3, %5 offset:1024 sc1\n\t"                   \
                         "global_load_dword %2, %4, %6 sc1" TAIL : C(pa[i]), C(pb[i]), C(pc[i])                                       \
                         : "v"(l16), "v"(lane * 4), "s"(kvbuf + c_ * 2048), "s"(auxbuf + c_ * 512) : "memory"); } while (0)
#define REC_BAD(i) (__builtin_amdgcn_ballot_w64(check_word(pa[i], pb[i], Z ? px[i][1] : 0u, salt) != (Z ? px[i][0] : pc[i])) != 0)
#pragma unroll
    for (int i = 0; i < 7; ++i) REC_LOAD(i, "", "=&v");
    if (w >= 4) __builtin_amdgcn_s_setprio(2);
    STAMP(14);
    const u32x4 id0 = lds_rd128(lds + LDS_ID + l16), id1 = lds_rd128(lds + LDS_ID + 1024 + l16);
    unsigned spin = 0;
#pragma unroll
    for (int i = 0; i < 7; ++i) {
      if (Z) asm volatile("s_waitcnt vmcnt(%3)" : "+v"(pa[i]), "+v"(pb[i]), "+v"(px[i]) : "n"(18 - 3 * i) : "memory");
      else asm volatile("s_waitcnt vmcnt(%3)" : "+v"(pa[i]), "+v"(pb[i]), "+v"(pc[i]) : "n"(18 - 3 * i) : "memory");
      if (__builtin_expect(REC_BAD(i), 0)) {
        do {
          COUNT_RETRY();
          __builtin_amdgcn_s_sleep(2);
          REC_LOAD(i, "\n\ts_waitcnt vmcnt(0)", "+v");
        } while (REC_BAD(i) && ++spin < SPIN_CAP);
      }
      kv = MFMA(as_frag(id0), as_frag(pa[i]), kv);
      kv = MFMA(as_frag(id1), as_frag(pb[i]), kv);
      if (Z) { const unsigned zu = px[i][1]; zown += __builtin_bit_cast(float, zu); }
    }
    STAMP(8);
    if (Z && lane < 32)
      *(LAS unsigned short*)(size_t)(lds + LDS_Z + mt * 64 + (((r >> 4) * 2 + ((r >> 2) & 1)) * 8 + ((r >> 3) & 1) * 4 + (r & 3)) * 2) = (unsigned short)pk_bf16(zown, 0.f);
    u32x4 d0, d1;
    pack_tile(kv, d0, d1);
    lds_wr128(kb + (mt * 4 + dt) * 2048, d0);
    lds_wr128(kb + (mt * 4 + dt) * 2048 + 1024, d1);
    arrive(lds + W_H + (w >> 2) * 4);
  };
  if (zwave) gather(BC<true>{}); else gather(BC<false>{});
  STAMP(9);

  {
    float inv;
#pragma unroll
    for (int half = 0; half < 2; ++half) {
      wait_ge(lds + W_H + half * 4, 4);
      if (half) STAMP(3); else STAMP(10);
      u32x4 zf[4], kf2[2][4];
      if (half == 0) {
#pragma unroll
        for (int u = 0; u < 4; ++u) zf[u] = lds_rd128(zb + u * 32);
      }
#pragma unroll
      for (int d = 0; d < 2; ++d) {
        SCHED_FENCE();
#pragma unroll
        for (int u = 0; u < 4; ++u) kf2[d][u] = lds_rd128(kb + ((u >> 1) * 4 + 2 * half + d) * 2048 + (u & 1) * 1024);
      }
      SCHED_FENCE();
      f32x16 den = {}, o[2] = {};
      if (half == 0) {
#pragma unroll
        for (int u = 0; u < 4; ++u) den = MFMA(as_frag(zf[u]), as_frag(xq[u]), den);
      }
#pragma unroll
      for (int d = 0; d < 2; ++d)
#pragma unroll
        for (int u = 0; u < 4; ++u) o[d] = MFMA(as_frag(kf2[d][u]), as_frag(xq[u]), o[d]);
      if (half == 0) inv = __builtin_amdgcn_rcpf(den[0] + 1e-6f);
#pragma unroll
      for (int d = 0; d < 2; ++d)
#pragma unroll
        for (int g = 0; g < 4; ++g) {
          const u32x2 pk = {pk_bf16(o[d][4 * g] * inv, o[d][4 * g + 1] * inv), pk_bf16(o[d][4 * g + 2] * inv, o[d][4 * g + 3] * inv)};
          lds_wr64(wb + (((8 * half + 4 * d + g) << 4) ^ wx), pk);
        }
      u32x4 x[4];
#pragma unroll
      for (int i = 0; i < 4; ++i) x[i] = lds_rd128(rb + i * 2048 + half * 128);
#pragma unroll
      for (int i = 0; i < 4; ++i) {
        if (i & 1) x[i] = (u32x4){x[i][2], x[i][3], x[i][0], x[i][1]};
        ST128_DEV(dst, so + (i >> 1) * 4096, x[i], (i & 1) * 2048 + half * 128);
      }
      if (half == 0) STAMP(2);
    }
  }
  if (chunk == 0 && w == 0 && lane == 0)
    asm volatile("global_store_dword %0, %1, %2 sc1" ::"v"(0u), "v"(tag), "s"(epw) : "memory");
  STAMP(11);
}

extern "C" void kernel_launch(void* const* d_in, const int* in_sizes, int n_in, void* d_out, int out_size, void* d_ws,
                              size_t ws_size, hipStream_t stream) {
  rfa_kernel<<<256, 576, 0, stream>>>((const char*)d_in[0], (const char*)d_in[1], (const char*)d_in[2], (const char*)d_in[3],
                                      (char*)d_out, (char*)d_ws);
}
```

```cpp
#include <hip/hip_runtime.h>
#include <hip/hip_bf16.h>
#include <stdint.h>

typedef __bf16 bf16x8 __attribute__((ext_vector_type(8)));
typedef __bf16 bf16x2 __attribute__((ext_vector_type(2)));
typedef short s16x4 __attribute__((ext_vector_type(4)));
typedef float f32x16 __attribute__((ext_vector_type(16)));
typedef float f32x4 __attribute__((ext_vector_type(4)));
typedef unsigned u32x4 __attribute__((ext_vector_type(4)));
typedef unsigned u32x2 __attribute__((ext_vector_type(2)));
#define LAS __attribute__((address_space(3)))
#define DI __device__ __forceinline__

#define LD_POL "nt"
#define WIN 24
#define PRO 40
#define GATE 16
#define ROT 1

constexpr int S = 2048;
constexpr int LDS_A = 0;
constexpr int LDS_B = 65536;
constexpr int LDS_OM = LDS_B + 49152;
constexpr int LDS_C = 131072;
constexpr int LDS_D = 155648;
constexpr int W_LANDED = LDS_D, W_OM = LDS_D + 4, W_PHI = LDS_D + 8, W_AF = LDS_D + 12, W_KV = LDS_D + 16, W_H = LDS_D + 20;
constexpr int LDS_Z = LDS_D + 128, LDS_ID = LDS_D + 256;
constexpr int P_K = 16, P_V = 80, P_Q = 144, P_END = 208;
constexpr size_t WS_EP = 0;
constexpr size_t WS_AUX = 65536;
constexpr size_t WS_KV = WS_AUX + 1048576;
constexpr unsigned SPIN_CAP = 1u << 18;

#define STAMP(i)
#define COUNT_RETRY()
#define MFMA(a, b, c) __builtin_amdgcn_mfma_f32_32x32x16_bf16((a), (b), (c), 0, 0, 0)
#define WAIT_VM(n) asm volatile("s_waitcnt vmcnt(%0)" ::"n"(n) : "memory")
#define SCHED_FENCE() __builtin_amdgcn_sched_barrier(0)

DI unsigned pk_bf16(float lo, float hi) { bf16x2 p; p[0] = (__bf16)lo; p[1] = (__bf16)hi; return __builtin_bit_cast(unsigned, p); }
DI bf16x8 as_frag(u32x4 v) { return __builtin_bit_cast(bf16x8, v); }
DI u32x4 lds_rd128(unsigned a) { return *(const LAS u32x4*)(size_t)a; }
DI void lds_wr128(unsigned a, u32x4 v) { *(LAS u32x4*)(size_t)a = v; }
DI void lds_wr64(unsigned a, u32x2 v) { *(LAS u32x2*)(size_t)a = v; }
DI float dot2(unsigned a, unsigned b, float c) { return __builtin_amdgcn_fdot2_f32_bf16(__builtin_bit_cast(bf16x2, a), __builtin_bit_cast(bf16x2, b), c, false); }
DI float add_halves(float x) {
  const unsigned u = __builtin_bit_cast(unsigned, x);
  const auto r = __builtin_amdgcn_permlane32_swap(u, u, false, false);
  return __builtin_bit_cast(float, (unsigned)r[0]) + __builtin_bit_cast(float, (unsigned)r[1]);
}
DI void pack_tile(const f32x16& t, u32x4& d0, u32x4& d1) {
#pragma unroll
  for (int j = 0; j < 4; ++j) { d0[j] = pk_bf16(t[2 * j], t[2 * j + 1]); d1[j] = pk_bf16(t[8 + 2 * j], t[8 + 2 * j + 1]); }
}
DI unsigned rot(unsigned x, int k) { return ROT ? __builtin_amdgcn_alignbit(x, x, k) : x; }
DI unsigned check_word(u32x4 a, u32x4 b, unsigned z, unsigned salt) {
  return ((a[0] + rot(a[1], 3) + rot(a[2], 5)) + (rot(a[3], 7) + rot(b[0], 9) + rot(b[1], 11))) + ((rot(b[2], 13) + rot(b[3], 15) + z) + salt);
}

#define DMA_QUAD(lds, g, v0, v1, v2, v3, POL)                                                   \
  asm volatile("s_mov_b32 m0, %0\n\ts_nop 0\n\t"                                                \
               "global_load_lds_dwordx4 %2, %1 offset:0 " POL "\n\t"                            \
               "global_load_lds_dwordx4 %3, %1 offset:1024 " POL "\n\t"                         \
               "global_load_lds_dwordx4 %4, %1 offset:2048 " POL "\n\t"                         \
               "global_load_lds_dwordx4 %5, %1 offset:3072 " POL                                \
               ::"s"(lds), "s"(g), "v"(v0), "v"(v1), "v"(v2), "v"(v3) : "memory")
#define ST128_DEV(g, voff, val, off)                                                            \
  asm volatile("global_store_dwordx4 %0, %1, %2 offset:%3 sc1\n\ts_nop 1" ::"v"(voff), "v"(val), "s"(g), "n"(off) : "memory")

DI void wait_ge(unsigned addr, unsigned need) {
  for (;;) {
    unsigned x;
    asm volatile("ds_read_b32 %0, %1\n\ts_waitcnt lgkmcnt(0)" : "=v"(x) : "v"(addr) : "memory");
    if ((unsigned)__builtin_amdgcn_readfirstlane(x) >= need) break;
    __builtin_amdgcn_s_sleep(1);
  }
}
DI void arrive(unsigned addr) {
  unsigned long long ex;
  asm volatile("s_mov_b64 %0, exec\n\ts_mov_b64 exec, 1\n\tds_add_u32 %1, %2\n\ts_mov_b64 exec, %0" : "=&s"(ex) : "v"(addr), "v"(1u) : "memory");
}
DI void post(unsigned addr, unsigned val) { asm volatile("ds_write_b32 %0, %1" ::"v"(addr), "v"(val) : "memory"); }

template <bool B> struct BC { static constexpr bool value = B; };

__global__ __launch_bounds__(576, 3) void rfa_kernel(const char* __restrict__ q, const char* __restrict__ k,
                                                     const char* __restrict__ v, const char* __restrict__ omega,
                                                     char* __restrict__ out, char* ws) {
  __shared__ __attribute__((aligned(16))) char smem[163840];
  STAMP(0);
  unsigned long long pc0;
  int left_a, left_b;
  asm volatile("s_getpc_b64 %0\n.Lrfa_pc:\n\ts_mov_b32 %1, .Lrfa_end_a-.Lrfa_pc\n\ts_mov_b32 %2, .Lrfa_end_b-.Lrfa_pc" : "=s"(pc0), "=s"(left_a), "=s"(left_b));
  const unsigned lds = (unsigned)(size_t)(LAS char*)smem;
  const int w = __builtin_amdgcn_readfirstlane(threadIdx.x >> 6);
  const unsigned lane = threadIdx.x & 63, r = lane & 31, h = lane >> 5;
  const int b = blockIdx.x, head = (b & 7) * 4 + (b >> 6), chunk = (b >> 3) & 7;
  const size_t row0 = (size_t)head * S + chunk * 256;
  const unsigned l16 = lane * 16;
  unsigned vs[4];
#pragma unroll
  for (int i = 0; i < 4; ++i) vs[i] = (lane >> 4) * 256 + (((lane & 15) ^ ((4 * i + (lane >> 4)) & 15)) << 4);
  const char* kk = k + row0 * 256;
  if (w == 8) {
#pragma unroll 1
    for (int j = 0; j < 4; ++j) DMA_QUAD(lds + LDS_OM + j * 4096, omega + j * 4096, vs[0], vs[1], vs[2], vs[3], "");
#pragma unroll 1
    for (int j = 0; j < (PRO - P_K) / 4; ++j) DMA_QUAD(lds + LDS_A + j * 4096, kk + j * 4096, vs[0], vs[1], vs[2], vs[3], LD_POL);
    post(lds + LDS_D + (lane & 7) * 4, 0u);
  }
  if (w == 7) {
    const unsigned pos = ((r >> 2) & 1) == h ? ((r >> 4) << 2) | (((r >> 3) & 1) << 1) | ((r >> 1) & 1) : 8u;
    const unsigned one = (r & 1) ? 0x3F800000u : 0x3F80u;
    u32x4 id0, id1;
#pragma unroll
    for (int j = 0; j < 4; ++j) { id0[j] = pos == j ? one : 0u; id1[j] = pos == 4 + j ? one : 0u; }
    lds_wr128(lds + LDS_ID + l16, id0);
    lds_wr128(lds + LDS_ID + 1024 + l16, id1);
  }
  asm volatile("s_waitcnt lgkmcnt(0)\n\ts_barrier" ::: "memory");

  if (w == 8) {
    const unsigned vv = (lane >> 4) * 256 + (((lane & 15) ^ ((lane >> 4) << 2)) << 4);
    const char* vsrc = v + row0 * 256;
    const char* qq = q + row0 * 256;
#pragma unroll
    for (int n = PRO - 16; n > WIN - 4; n -= 4) {
      WAIT_VM(n);
      post(lds + W_LANDED, PRO - n);
    }
    unsigned done = PRO - WIN;
    auto run = [&](int quads, unsigned dst, const char* src, unsigned v0, unsigned v1, unsigned v2, unsigned v3) {
#pragma unroll 1
      for (int j = 0; j < quads; ++j) {
        DMA_QUAD(dst, src, v0, v1, v2, v3, LD_POL);
        dst += 4096; src += 4096; done += 4;
        WAIT_VM(WIN);
        post(lds + W_LANDED, done);
      }
    };
    run((P_V - PRO) / 4, lds + LDS_A + (PRO - P_K) * 1024, kk + (PRO - P_K) * 1024, vs[0], vs[1], vs[2], vs[3]);
    STAMP(1);
    run(12, lds + LDS_B, vsrc, vv, vv, vv, vv);
    wait_ge(lds + W_OM, 8);
    run(4, lds + LDS_B + 49152, vsrc + 49152, vv, vv, vv, vv);
    STAMP(2);
    run(6, lds + LDS_C, qq, vs[0], vs[1], vs[2], vs[3]);
    wait_ge(lds + W_AF, 8);
    run(10, lds + LDS_A, qq + 24576, vs[0], vs[1], vs[2], vs[3]);
    STAMP(3);
#pragma unroll
    for (int n = WIN - 4; n >= 0; n -= 4) {
      WAIT_VM(n);
      post(lds + W_LANDED, P_END - n);
    }
    STAMP(4);
    asm volatile(".Lrfa_end_a:");
    return;
  }

  const int mt = (w >> 1) & 1, dt = (w & 1) | ((w >> 2) << 1);
  const bool zwave = dt == 0;
  char* epw = ws + WS_EP + head * 128;
  char* auxbuf = ws + WS_AUX + (size_t)(head * 8 + w) * 4096;
  char* kvbuf = ws + WS_KV + (size_t)(head * 8 + w) * 16384;
  __builtin_amdgcn_s_setprio(1);
  unsigned ep;
  asm volatile("global_load_dword %0, %1, %2 sc1" : "=v"(ep) : "v"(0u), "s"(epw) : "memory");
  unsigned warm, warm2;
  asm volatile("s_load_dword %0, %2, 0x0\n\ts_load_dword %1, %2, 0x40" : "=&s"(warm), "=&s"(warm2) : "s"(pc0 + min((b >> 3) * 8 + w, (max(left_a, left_b) >> 7) - 1) * 128) : "memory");
  STAMP(1);

  const unsigned xr = r * 256 + ((h ^ (r & 15)) << 4);
  u32x4 om[2][8];
  wait_ge(lds + W_LANDED, P_K);
  asm volatile("" ::"s"(warm), "s"(warm2));
#pragma unroll
  for (int i = 0; i < 2; ++i)
#pragma unroll
    for (int t = 0; t < 8; ++t) om[i][t] = lds_rd128(((lds + LDS_OM + xr) ^ (t << 5)) + i * 8192);
  arrive(lds + W_OM);

  const float scale = 0.29730177875068026f;
  const float c1 = scale * 1.4426950408889634f;
  const float c2 = -0.5f * scale * scale * 1.4426950408889634f;

  {
    const unsigned slotA = lds + LDS_A + w * 8192;
    wait_ge(lds + W_LANDED, P_K + 8 * (w + 1));
    u32x4 kf[8];
    unsigned ka = slotA + xr;
    asm volatile("" : "+v"(ka));
#pragma unroll
    for (int t = 0; t < 8; ++t) kf[t] = lds_rd128(ka ^ (t << 5));
    SCHED_FENCE();
    f32x16 acc[2] = {};
    float ss[4] = {};
#pragma unroll
    for (int t = 0; t < 8; ++t)
#pragma unroll
      for (int i = 0; i < 4; ++i) ss[i] = dot2(kf[t][i], kf[t][i], ss[i]);
#pragma unroll
    for (int t = 0; t < 8; ++t) acc[0] = MFMA(as_frag(kf[t]), as_frag(om[0][t]), acc[0]);
    const float bias = add_halves((ss[0] + ss[1]) + (ss[2] + ss[3])) * c2 - 3.0f;
    *(LAS float*)(size_t)(slotA + 4096 + r * 4) = bias;
    f32x4 bz[4];
#pragma unroll
    for (int g = 0; g < 4; ++g) bz[g] = *(const LAS f32x4*)(size_t)(slotA + 4096 + (8 * g + 4 * h) * 4);
#pragma unroll
    for (int t = 0; t < 8; ++t) acc[1] = MFMA(as_frag(kf[t]), as_frag(om[1][t]), acc[1]);
#pragma unroll
    for (int i = 0; i < 2; ++i)
#pragma unroll
      for (int s = 0; s < 2; ++s) {
        u32x4 f;
#pragma unroll
        for (int j = 0; j < 4; ++j) {
          const int e = 8 * s + 2 * j;
          const float p0 = __builtin_amdgcn_exp2f(acc[i][e] * c1 + bz[e >> 2][e & 3]) + 1e-6f;
          const float p1 = __builtin_amdgcn_exp2f(acc[i][e + 1] * c1 + bz[(e + 1) >> 2][(e + 1) & 3]) + 1e-6f;
          f[j] = pk_bf16(p0, p1);
        }
        lds_wr128(slotA + (i * 2 + s) * 1024 + l16, f);
      }
  }
  arrive(lds + W_PHI);
  asm volatile("s_waitcnt vmcnt(0)" : "+v"(ep)::"memory");
  const unsigned tag = __builtin_amdgcn_readfirstlane(ep) + 1;
  const unsigned salt = tag + lane * 0x9E3779B1u;
  wait_ge(lds + W_PHI, 8);
  STAMP(4);

  f32x16 kv = {};
  float zown = 0.f;
  {
    const unsigned ab = lds + LDS_A + mt * 2048 + l16;
    u32x4 af[16];
#pragma unroll
    for (int ks = 0; ks < 10; ++ks) af[ks] = lds_rd128(ab + (ks >> 1) * 8192 + (ks & 1) * 1024);
    arrive(lds + W_AF);
    float zs[4] = {};
    const unsigned qq = (lane & 15) >> 2, p = lane & 3, blk = (lane >> 4) & 1;
    const unsigned trb = lds + LDS_B + (4 * h + qq) * 256 + (((4 * dt + 2 * blk + (p >> 1)) ^ (qq << 2)) << 4) + 8 * (p & 1);
#pragma unroll
    for (int c = 0; c < 4; ++c) {
      if (c == 1 || c == 2) {
#pragma unroll
        for (int ks = (c == 1 ? 10 : 12); ks < (c == 1 ? 12 : 16); ++ks) af[ks] = lds_rd128(ab + (ks >> 1) * 8192 + (ks & 1) * 1024);
      }
      if (zwave && c < 3) {
#pragma unroll
        for (int ks = (c == 0 ? 0 : 4 * c + 4); ks < 4 * c + 8; ++ks)
#pragma unroll
          for (int i = 0; i < 4; ++i) zs[i] = dot2(af[ks][i], 0x3F803F80u, zs[i]);
        if (c == 2) zown = add_halves((zs[0] + zs[1]) + (zs[2] + zs[3]));
      }
      SCHED_FENCE();
      wait_ge(lds + W_LANDED, P_V + 16 * (c + 1));
      if (c == 3) STAMP(12);
      s16x4 vf[8];
#pragma unroll
      for (int u = 0; u < 8; ++u) vf[u] = __builtin_amdgcn_ds_read_tr16_b64_v4i16((LAS s16x4*)(size_t)(trb + (64 * c + 8 * u) * 256));
      SCHED_FENCE();
#pragma unroll
      for (int u = 0; u < 4; ++u)
        kv = MFMA(as_frag(af[4 * c + u]), __builtin_bit_cast(bf16x8, __builtin_shufflevector(vf[2 * u], vf[2 * u + 1], 0, 1, 2, 3, 4, 5, 6, 7)), kv);
    }
    arrive(lds + W_KV);
  }
  {
    u32x4 d0, d1;
    pack_tile(kv, d0, d1);
    ST128_DEV(kvbuf + chunk * 2048, l16, d0, 0);
    ST128_DEV(kvbuf + chunk * 2048, l16, d1, 1024);
    const unsigned zb = __builtin_bit_cast(unsigned, zown);
    const u32x2 ax = {check_word(d0, d1, zb, salt), zb};
    if (zwave) asm volatile("global_store_dwordx2 %0, %1, %2 sc1" ::"v"(lane * 8), "v"(ax), "s"(auxbuf + chunk * 512) : "memory");
    else asm volatile("global_store_dword %0, %1, %2 sc1" ::"v"(lane * 4), "v"(ax[0]), "s"(auxbuf + chunk * 512) : "memory");
  }
  STAMP(5);

  if (w >= 4) __builtin_amdgcn_s_setprio(2);
  const unsigned slotQ = w < 3 ? lds + LDS_C + w * 8192 : lds + LDS_A + (w - 3) * 8192;
  u32x4 xq[4];
  {
    wait_ge(lds + W_LANDED, P_Q + 8 * (w + 1));
    STAMP(7);
    u32x4 qf[8];
    unsigned qa = slotQ + xr;
    asm volatile("" : "+v"(qa));
#pragma unroll
    for (int t = 0; t < 8; ++t) qf[t] = lds_rd128(qa ^ (t << 5));
    SCHED_FENCE();
    f32x16 acc[2] = {};
    float ss[4] = {};
#pragma unroll
    for (int t = 0; t < 8; ++t)
#pragma unroll
      for (int i = 0; i < 4; ++i) ss[i] = dot2(qf[t][i], qf[t][i], ss[i]);
#pragma unroll
    for (int t = 0; t < 8; ++t) acc[0] = MFMA(as_frag(om[0][t]), as_frag(qf[t]), acc[0]);
    const float bias = add_halves((ss[0] + ss[1]) + (ss[2] + ss[3])) * c2 - 3.0f;
#pragma unroll
    for (int t = 0; t < 8; ++t) acc[1] = MFMA(as_frag(om[1][t]), as_frag(qf[t]), acc[1]);
#pragma unroll
    for (int u = 0; u < 4; ++u)
#pragma unroll
      for (int j = 0; j < 4; ++j) {
        const int e = 8 * (u & 1) + 2 * j;
        xq[u][j] = pk_bf16(__builtin_amdgcn_exp2f(acc[u >> 1][e] * c1 + bias) + 1e-6f, __builtin_amdgcn_exp2f(acc[u >> 1][e + 1] * c1 + bias) + 1e-6f);
      }
    asm volatile("" : "+v"(xq[0]), "+v"(xq[1]), "+v"(xq[2]), "+v"(xq[3]));
    SCHED_FENCE();
  }
  unsigned wb = slotQ + r * 256 + 8 * (h ^ ((r >> 3) & 1)), wx = (r & 7) << 4;
  char* dst = out + (row0 + w * 32) * 256;
  unsigned so = (lane >> 3) * 256 + (lane & 7) * 16;
  unsigned rb = slotQ + (lane >> 3) * 256 + (((lane & 7) ^ (lane >> 3)) << 4);
  unsigned kb = lds + LDS_B + l16, zb = lds + LDS_Z + h * 16;
  asm volatile("" : "+v"(wb), "+v"(wx), "+v"(so), "+v"(rb), "+v"(kb), "+v"(zb), "+s"(dst));

  __builtin_amdgcn_s_setprio(1);
  wait_ge(lds + W_KV, 8);
  wait_ge(lds + W_LANDED, P_END - GATE);
  STAMP(6);
  auto gather = [&](auto with_z) {
    constexpr bool Z = decltype(with_z)::value;
    u32x4 pa[7], pb[7];
    unsigned pc[7];
    u32x2 px[7];
#define REC_LOAD(i, TAIL, C)                                                                                                          \
  do { const int c_ = (chunk + 1 + (i)) & 7;                                                                                          \
       if (Z) asm volatile("global_load_dwordx4 %0, %3, %5 sc1\n\tglobal_load_dwordx4 %1, %3, %5 offset:1024 sc1\n\t"                 \
                           "global_load_dwordx2 %2, %4, %6 sc1" TAIL : C(pa[i]), C(pb[i]), C(px[i])                                   \
                           : "v"(l16), "v"(lane * 8), "s"(kvbuf + c_ * 2048), "s"(auxbuf + c_ * 512) : "memory");                     \
       else asm volatile("global_load_dwordx4 %0, %3, %5 sc1\n\tglobal_load_dwordx4 %1, %3, %5 offset:1024 sc1\n\t"                   \
                         "global_load_dword %2, %4, %6 sc1" TAIL : C(pa[i]), C(pb[i]), C(pc[i])                                       \
                         : "v"(l16), "v"(lane * 4), "s"(kvbuf + c_ * 2048), "s"(auxbuf + c_ * 512) : "memory"); } while (0)
#define REC_BAD(i) (__builtin_amdgcn_ballot_w64(check_word(pa[i], pb[i], Z ? px[i][1] : 0u, salt) != (Z ? px[i][0] : pc[i])) != 0)
#pragma unroll
    for (int i = 0; i < 7; ++i) REC_LOAD(i, "", "=&v");
    if (w >= 4) __builtin_amdgcn_s_setprio(2);
    STAMP(14);
    const u32x4 id0 = lds_rd128(lds + LDS_ID + l16), id1 = lds_rd128(lds + LDS_ID + 1024 + l16);
    unsigned spin = 0;
#pragma unroll
    for (int i = 0; i < 7; ++i) {
      if (Z) asm volatile("s_waitcnt vmcnt(%3)" : "+v"(pa[i]), "+v"(pb[i]), "+v"(px[i]) : "n"(18 - 3 * i) : "memory");
      else asm volatile("s_waitcnt vmcnt(%3)" : "+v"(pa[i]), "+v"(pb[i]), "+v"(pc[i]) : "n"(18 - 3 * i) : "memory");
      if (__builtin_expect(REC_BAD(i), 0)) {
        do {
          COUNT_RETRY();
          __builtin_amdgcn_s_sleep(2);
          REC_LOAD(i, "\n\ts_waitcnt vmcnt(0)", "+v");
        } while (REC_BAD(i) && ++spin < SPIN_CAP);
      }
      kv = MFMA(as_frag(id0), as_frag(pa[i]), kv);
      kv = MFMA(as_frag(id1), as_frag(pb[i]), kv);
      if (Z) { const unsigned zu = px[i][1]; zown += __builtin_bit_cast(float, zu); }
    }
    STAMP(8);
    if (Z && lane < 32)
      *(LAS unsigned short*)(size_t)(lds + LDS_Z + mt * 64 + (((r >> 4) * 2 + ((r >> 2) & 1)) * 8 + ((r >> 3) & 1) * 4 + (r & 3)) * 2) = (unsigned short)pk_bf16(zown, 0.f);
    u32x4 d0, d1;
    pack_tile(kv, d0, d1);
    lds_wr128(kb + (mt * 4 + dt) * 2048, d0);
    lds_wr128(kb + (mt * 4 + dt) * 2048 + 1024, d1);
    arrive(lds + W_H + (w >> 2) * 4);
  };
  if (zwave) gather(BC<true>{}); else gather(BC<false>{});
  STAMP(9);

  {
    float inv;
#pragma unroll
    for (int half = 0; half < 2; ++half) {
      wait_ge(lds + W_H + half * 4, 4);
      if (half) STAMP(3); else STAMP(10);
      u32x4 zf[4], kf2[2][4];
      if (half == 0) {
#pragma unroll
        for (int u = 0; u < 4; ++u) zf[u] = lds_rd128(zb + u * 32);
      }
#pragma unroll
      for (int d = 0; d < 2; ++d) {
        SCHED_FENCE();
#pragma unroll
        for (int u = 0; u < 4; ++u) kf2[d][u] = lds_rd128(kb + ((u >> 1) * 4 + 2 * half + d) * 2048 + (u & 1) * 1024);
      }
      SCHED_FENCE();
      f32x16 den = {}, o[2] = {};
      if (half == 0) {
#pragma unroll
        for (int u = 0; u < 4; ++u) den = MFMA(as_frag(zf[u]), as_frag(xq[u]), den);
      }
#pragma unroll
      for (int d = 0; d < 2; ++d)
#pragma unroll
        for (int u = 0; u < 4; ++u) o[d] = MFMA(as_frag(kf2[d][u]), as_frag(xq[u]), o[d]);
      if (half == 0) inv = __builtin_amdgcn_rcpf(den[0] + 1e-6f);
#pragma unroll
      for (int d = 0; d < 2; ++d)
#pragma unroll
        for (int g = 0; g < 4; ++g) {
          const u32x2 pk = {pk_bf16(o[d][4 * g] * inv, o[d][4 * g + 1] * inv), pk_bf16(o[d][4 * g + 2] * inv, o[d][4 * g + 3] * inv)};
          lds_wr64(wb + (((8 * half + 4 * d + g) << 4) ^ wx), pk);
        }
      u32x4 x[4];
#pragma unroll
      for (int i = 0; i < 4; ++i) x[i] = lds_rd128(rb + i * 2048 + half * 128);
#pragma unroll
      for (int i = 0; i < 4; ++i) {
        if (i & 1) x[i] = (u32x4){x[i][2], x[i][3], x[i][0], x[i][1]};
        ST128_DEV(dst, so + (i >> 1) * 4096, x[i], (i & 1) * 2048 + half * 128);
      }
      if (half == 0) STAMP(2);
    }
  }
  if (chunk == 0 && w == 0 && lane == 0)
    asm volatile("global_store_dword %0, %1, %2 sc1" ::"v"(0u), "v"(tag), "s"(epw) : "memory");
  STAMP(11);
  asm volatile(".Lrfa_end_b:");
}

extern "C" void kernel_launch(void* const* d_in, const int* in_sizes, int n_in, void* d_out, int out_size, void* d_ws,
                              size_t ws_size, hipStream_t stream) {
  rfa_kernel<<<256, 576, 0, stream>>>((const char*)d_in[0], (const char*)d_in[1], (const char*)d_in[2], (const char*)d_in[3],
                                      (char*)d_out, (char*)d_ws);
}
```

```cpp
#include <hip/hip_runtime.h>
#include <hip/hip_bf16.h>
#include <stdint.h>

typedef __bf16 bf16x8 __attribute__((ext_vector_type(8)));
typedef __bf16 bf16x2 __attribute__((ext_vector_type(2)));
typedef short s16x4 __attribute__((ext_vector_type(4)));
typedef float f32x16 __attribute__((ext_vector_type(16)));
typedef float f32x4 __attribute__((ext_vector_type(4)));
typedef unsigned u32x4 __attribute__((ext_vector_type(4)));
typedef unsigned u32x2 __attribute__((ext_vector_type(2)));
#define LAS __attribute__((address_space(3)))
#define DI __device__ __forceinline__

#define LD_POL "nt"
#define WIN 24
#define PRO 40
#define GATE 16
#define ROT 1

constexpr int S = 2048;
constexpr int LDS_A = 0;
constexpr int LDS_B = 65536;
constexpr int LDS_OM = LDS_B + 49152;
constexpr int LDS_C = 131072;
constexpr int LDS_D = 155648;
constexpr int W_LANDED = LDS_D, W_OM = LDS_D + 4, W_PHI = LDS_D + 8, W_AF = LDS_D + 12, W_KV = LDS_D + 16, W_H = LDS_D + 20;
constexpr int LDS_Z = LDS_D + 128, LDS_ID = LDS_D + 256;
constexpr int P_K = 16, P_V = 80, P_Q = 144, P_END = 208;
constexpr size_t WS_EP = 0;
constexpr size_t WS_AUX = 65536;
constexpr size_t WS_KV = WS_AUX + 1048576;
constexpr unsigned SPIN_CAP = 1u << 18;

#define STAMP(i)
#define COUNT_RETRY()
#define MFMA(a, b, c) __builtin_amdgcn_mfma_f32_32x32x16_bf16((a), (b), (c), 0, 0, 0)
#define WAIT_VM(n) asm volatile("s_waitcnt vmcnt(%0)" ::"n"(n) : "memory")
#define SCHED_FENCE() __builtin_amdgcn_sched_barrier(0)

DI unsigned pk_bf16(float lo, float hi) { bf16x2 p; p[0] = (__bf16)lo; p[1] = (__bf16)hi; return __builtin_bit_cast(unsigned, p); }
DI bf16x8 as_frag(u32x4 v) { return __builtin_bit_cast(bf16x8, v); }
DI u32x4 lds_rd128(unsigned a) { return *(const LAS u32x4*)(size_t)a; }
DI void lds_wr128(unsigned a, u32x4 v) { *(LAS u32x4*)(size_t)a = v; }
DI void lds_wr64(unsigned a, u32x2 v) { *(LAS u32x2*)(size_t)a = v; }
DI float dot2(unsigned a, unsigned b, float c) { return __builtin_amdgcn_fdot2_f32_bf16(__builtin_bit_cast(bf16x2, a), __builtin_bit_cast(bf16x2, b), c, false); }
DI float add_halves(float x) {
  const unsigned u = __builtin_bit_cast(unsigned, x);
  const auto r = __builtin_amdgcn_permlane32_swap(u, u, false, false);
  return __builtin_bit_cast(float, (unsigned)r[0]) + __builtin_bit_cast(float, (unsigned)r[1]);
}
DI void pack_tile(const f32x16& t, u32x4& d0, u32x4& d1) {
#pragma unroll
  for (int j = 0; j < 4; ++j) { d0[j] = pk_bf16(t[2 * j], t[2 * j + 1]); d1[j] = pk_bf16(t[8 + 2 * j], t[8 + 2 * j + 1]); }
}
DI unsigned rot(unsigned x, int k) { return ROT ? __builtin_amdgcn_alignbit(x, x, k) : x; }
DI unsigned check_word(u32x4 a, u32x4 b, unsigned z, unsigned salt) {
  return ((a[0] + rot(a[1], 3) + rot(a[2], 5)) + (rot(a[3], 7) + rot(b[0], 9) + rot(b[1], 11))) + ((rot(b[2], 13) + rot(b[3], 15) + z) + salt);
}

#define DMA_QUAD(lds, g, v0, v1, v2, v3, POL)                                                   \
  asm volatile("s_mov_b32 m0, %0\n\ts_nop 0\n\t"                                                \
               "global_load_lds_dwordx4 %2, %1 offset:0 " POL "\n\t"                            \
               "global_load_lds_dwordx4 %3, %1 offset:1024 " POL "\n\t"                         \
               "global_load_lds_dwordx4 %4, %1 offset:2048 " POL "\n\t"                         \
               "global_load_lds_dwordx4 %5, %1 offset:3072 " POL                                \
               ::"s"(lds), "s"(g), "v"(v0), "v"(v1), "v"(v2), "v"(v3) : "memory")
#define ST128_DEV(g, voff, val, off)                                                            \
  asm volatile("global_store_dwordx4 %0, %1, %2 offset:%3 sc1\n\ts_nop 1" ::"v"(voff), "v"(val), "s"(g), "n"(off) : "memory")

DI void wait_ge(unsigned addr, unsigned need) {
  for (;;) {
    unsigned x;
    asm volatile("ds_read_b32 %0, %1\n\ts_waitcnt lgkmcnt(0)" : "=v"(x) : "v"(addr) : "memory");
    if ((unsigned)__builtin_amdgcn_readfirstlane(x) >= need) break;
    __builtin_amdgcn_s_sleep(1);
  }
}
DI void arrive(unsigned addr) {
  unsigned long long ex;
  asm volatile("s_mov_b64 %0, exec\n\ts_mov_b64 exec, 1\n\tds_add_u32 %1, %2\n\ts_mov_b64 exec, %0" : "=&s"(ex) : "v"(addr), "v"(1u) : "memory");
}
DI void post(unsigned addr, unsigned val) { asm volatile("ds_write_b32 %0, %1" ::"v"(addr), "v"(val) : "memory"); }

__global__ __launch_bounds__(576, 3) void rfa_kernel(const char* __restrict__ q, const char* __restrict__ k,
                                                     const char* __restrict__ v, const char* __restrict__ omega,
                                                     char* __restrict__ out, char* ws) {
  __shared__ __attribute__((aligned(16))) char smem[163840];
  STAMP(0);
  unsigned long long pc0;
  int left_a, left_b;
  asm volatile("s_getpc_b64 %0\n.Lrfa_pc:\n\ts_mov_b32 %1, .Lrfa_end_a-.Lrfa_pc\n\ts_mov_b32 %2, .Lrfa_end_b-.Lrfa_pc" : "=s"(pc0), "=s"(left_a), "=s"(left_b));
  const unsigned lds = (unsigned)(size_t)(LAS char*)smem;
  const int w = __builtin_amdgcn_readfirstlane(threadIdx.x >> 6);
  const unsigned lane = threadIdx.x & 63, r = lane & 31, h = lane >> 5;
  const int b = blockIdx.x, head = (b & 7) * 4 + (b >> 6), chunk = (b >> 3) & 7;
  const size_t row0 = (size_t)head * S + chunk * 256;
  const unsigned l16 = lane * 16;
  unsigned vs[4];
#pragma unroll
  for (int i = 0; i < 4; ++i) vs[i] = (lane >> 4) * 256 + (((lane & 15) ^ ((4 * i + (lane >> 4)) & 15)) << 4);
  const char* kk = k + row0 * 256;
  if (w == 8) {
#pragma unroll 1
    for (int j = 0; j < 4; ++j) DMA_QUAD(lds + LDS_OM + j * 4096, omega + j * 4096, vs[0], vs[1], vs[2], vs[3], "");
#pragma unroll 1
    for (int j = 0; j < (PRO - P_K) / 4; ++j) DMA_QUAD(lds + LDS_A + j * 4096, kk + j * 4096, vs[0], vs[1], vs[2], vs[3], LD_POL);
    post(lds + LDS_D + (lane & 7) * 4, 0u);
  }
  if (w == 7) {
    const unsigned pos = ((r >> 2) & 1) == h ? ((r >> 4) << 2) | (((r >> 3) & 1) << 1) | ((r >> 1) & 1) : 8u;
    const unsigned one = (r & 1) ? 0x3F800000u : 0x3F80u;
    u32x4 id0, id1;
#pragma unroll
    for (int j = 0; j < 4; ++j) { id0[j] = pos == j ? one : 0u; id1[j] = pos == 4 + j ? one : 0u; }
    lds_wr128(lds + LDS_ID + l16, id0);
    lds_wr128(lds + LDS_ID + 1024 + l16, id1);
  }
  asm volatile("s_waitcnt lgkmcnt(0)\n\ts_barrier" ::: "memory");

  if (w == 8) {
    const unsigned vv = (lane >> 4) * 256 + (((lane & 15) ^ ((lane >> 4) << 2)) << 4);
    const char* vsrc = v + row0 * 256;
    const char* qq = q + row0 * 256;
#pragma unroll
    for (int n = PRO - 16; n > WIN - 4; n -= 4) {
      WAIT_VM(n);
      post(lds + W_LANDED, PRO - n);
    }
    unsigned done = PRO - WIN;
    auto run = [&](int quads, unsigned dst, const char* src, unsigned v0, unsigned v1, unsigned v2, unsigned v3) {
#pragma unroll 1
      for (int j = 0; j < quads; ++j) {
        DMA_QUAD(dst, src, v0, v1, v2, v3, LD_POL);
        dst += 4096; src += 4096; done += 4;
        WAIT_VM(WIN);
        post(lds + W_LANDED, done);
      }
    };
    run((P_V - PRO) / 4, lds + LDS_A + (PRO - P_K) * 1024, kk + (PRO - P_K) * 1024, vs[0], vs[1], vs[2], vs[3]);
    STAMP(1);
    run(12, lds + LDS_B, vsrc, vv, vv, vv, vv);
    wait_ge(lds + W_OM, 8);
    run(4, lds + LDS_B + 49152, vsrc + 49152, vv, vv, vv, vv);
    STAMP(2);
    run(6, lds + LDS_C, qq, vs[0], vs[1], vs[2], vs[3]);
    wait_ge(lds + W_AF, 8);
    run(10, lds + LDS_A, qq + 24576, vs[0], vs[1], vs[2], vs[3]);
    STAMP(3);
#pragma unroll
    for (int n = WIN - 4; n >= 0; n -= 4) {
      WAIT_VM(n);
      post(lds + W_LANDED, P_END - n);
    }
    STAMP(4);
    asm volatile(".Lrfa_end_a:");
    return;
  }

  const int mt = (w >> 1) & 1, dt = (w & 1) | ((w >> 2) << 1);
  const bool zwave = dt == 0;
  char* epw = ws + WS_EP + head * 128;
  char* auxbuf = ws + WS_AUX + (size_t)(head * 8 + w) * 4096;
  char* kvbuf = ws + WS_KV + (size_t)(head * 8 + w) * 16384;
  __builtin_amdgcn_s_setprio(1);
  unsigned ep;
  asm volatile("global_load_dword %0, %1, %2 sc1" : "=v"(ep) : "v"(0u), "s"(epw) : "memory");
  unsigned touch;
  asm volatile("global_load_dword %0, %1, %2 nt" : "=v"(touch) : "v"(0u),
               "s"(w < 2 ? v + row0 * 256 + w * 32768 : w < 4 ? q + row0 * 256 + (w - 2) * 32768 : w < 6 ? out + row0 * 256 + (w - 4) * 32768 : w == 6 ? kvbuf : auxbuf) : "memory");
  unsigned warm, warm2;
  asm volatile("s_load_dword %0, %2, 0x0\n\ts_load_dword %1, %2, 0x40" : "=&s"(warm), "=&s"(warm2) : "s"(pc0 + min((b >> 3) * 8 + w, (max(left_a, left_b) >> 7) - 1) * 128) : "memory");
  STAMP(1);

  const unsigned xr = r * 256 + ((h ^ (r & 15)) << 4);
  u32x4 om[2][8];
  wait_ge(lds + W_LANDED, P_K);
  asm volatile("" ::"s"(warm), "s"(warm2));
#pragma unroll
  for (int i = 0; i < 2; ++i)
#pragma unroll
    for (int t = 0; t < 8; ++t) om[i][t] = lds_rd128(((lds + LDS_OM + xr) ^ (t << 5)) + i * 8192);
  arrive(lds + W_OM);

  const float scale = 0.29730177875068026f;
  const float c1 = scale * 1.4426950408889634f;
  const float c2 = -0.5f * scale * scale * 1.4426950408889634f;

  {
    const unsigned slotA = lds + LDS_A + w * 8192;
    wait_ge(lds + W_LANDED, P_K + 8 * (w + 1));
    u32x4 kf[8];
    unsigned ka = slotA + xr;
    asm volatile("" : "+v"(ka));
#pragma unroll
    for (int t = 0; t < 8; ++t) kf[t] = lds_rd128(ka ^ (t << 5));
    SCHED_FENCE();
    f32x16 acc[2] = {};
    float ss[4] = {};
#pragma unroll
    for (int t = 0; t < 8; ++t)
#pragma unroll
      for (int i = 0; i < 4; ++i) ss[i] = dot2(kf[t][i], kf[t][i], ss[i]);
#pragma unroll
    for (int t = 0; t < 8; ++t) acc[0] = MFMA(as_frag(kf[t]), as_frag(om[0][t]), acc[0]);
    const float bias = add_halves((ss[0] + ss[1]) + (ss[2] + ss[3])) * c2 - 3.0f;
    *(LAS float*)(size_t)(slotA + 4096 + r * 4) = bias;
    f32x4 bz[4];
#pragma unroll
    for (int g = 0; g < 4; ++g) bz[g] = *(const LAS f32x4*)(size_t)(slotA + 4096 + (8 * g + 4 * h) * 4);
#pragma unroll
    for (int t = 0; t < 8; ++t) acc[1] = MFMA(as_frag(kf[t]), as_frag(om[1][t]), acc[1]);
#pragma unroll
    for (int i = 0; i < 2; ++i)
#pragma unroll
      for (int s = 0; s < 2; ++s) {
        u32x4 f;
#pragma unroll
        for (int j = 0; j < 4; ++j) {
          const int e = 8 * s + 2 * j;
          const float p0 = __builtin_amdgcn_exp2f(acc[i][e] * c1 + bz[e >> 2][e & 3]) + 1e-6f;
          const float p1 = __builtin_amdgcn_exp2f(acc[i][e + 1] * c1 + bz[(e + 1) >> 2][(e + 1) & 3]) + 1e-6f;
          f[j] = pk_bf16(p0, p1);
        }
        lds_wr128(slotA + (i * 2 + s) * 1024 + l16, f);
      }
  }
  arrive(lds + W_PHI);
  asm volatile("s_waitcnt vmcnt(0)" : "+v"(ep), "+v"(touch)::"memory");
  const unsigned tag = __builtin_amdgcn_readfirstlane(ep) + 1;
  const unsigned salt = tag + lane * 0x9E3779B1u;
  wait_ge(lds + W_PHI, 8);
  STAMP(4);

  f32x16 kv = {};
  float zown = 0.f;
  {
    const unsigned ab = lds + LDS_A + mt * 2048 + l16;
    u32x4 af[16];
#pragma unroll
    for (int ks = 0; ks < 10; ++ks) af[ks] = lds_rd128(ab + (ks >> 1) * 8192 + (ks & 1) * 1024);
    arrive(lds + W_AF);
    float zs[4] = {};
    const unsigned qq = (lane & 15) >> 2, p = lane & 3, blk = (lane >> 4) & 1;
    const unsigned trb = lds + LDS_B + (4 * h + qq) * 256 + (((4 * dt + 2 * blk + (p >> 1)) ^ (qq << 2)) << 4) + 8 * (p & 1);
#pragma unroll
    for (int c = 0; c < 4; ++c) {
      if (c == 1 || c == 2) {
#pragma unroll
        for (int ks = (c == 1 ? 10 : 12); ks < (c == 1 ? 12 : 16); ++ks) af[ks] = lds_rd128(ab + (ks >> 1) * 8192 + (ks & 1) * 1024);
      }
      if (zwave && c < 3) {
#pragma unroll
        for (int ks = (c == 0 ? 0 : 4 * c + 4); ks < 4 * c + 8; ++ks)
#pragma unroll
          for (int i = 0; i < 4; ++i) zs[i] = dot2(af[ks][i], 0x3F803F80u, zs[i]);
        if (c == 2) zown = add_halves((zs[0] + zs[1]) + (zs[2] + zs[3]));
      }
      SCHED_FENCE();
      wait_ge(lds + W_LANDED, P_V + 16 * (c + 1));
      if (c == 3) STAMP(12);
      s16x4 vf[8];
#pragma unroll
      for (int u = 0; u < 8; ++u) vf[u] = __builtin_amdgcn_ds_read_tr16_b64_v4i16((LAS s16x4*)(size_t)(trb + (64 * c + 8 * u) * 256));
      SCHED_FENCE();
#pragma unroll
      for (int u = 0; u < 4; ++u)
        kv = MFMA(as_frag(af[4 * c + u]), __builtin_bit_cast(bf16x8, __builtin_shufflevector(vf[2 * u], vf[2 * u + 1], 0, 1, 2, 3, 4, 5, 6, 7)), kv);
    }
    arrive(lds + W_KV);
  }
  {
    u32x4 d0, d1;
    pack_tile(kv, d0, d1);
    ST128_DEV(kvbuf + chunk * 2048, l16, d0, 0);
    ST128_DEV(kvbuf + chunk * 2048, l16, d1, 1024);
    const unsigned zb = __builtin_bit_cast(unsigned, zown);
    const u32x2 ax = {check_word(d0, d1, zb, salt), zb};
    if (zwave) asm volatile("global_store_dwordx2 %0, %1, %2 sc1" ::"v"(lane * 8), "v"(ax), "s"(auxbuf + chunk * 512) : "memory");
    else asm volatile("global_store_dword %0, %1, %2 sc1" ::"v"(lane * 4), "v"(ax[0]), "s"(auxbuf + chunk * 512) : "memory");
  }
  STAMP(5);

  if (w >= 4) __builtin_amdgcn_s_setprio(2);
  const unsigned slotQ = w < 3 ? lds + LDS_C + w * 8192 : lds + LDS_A + (w - 3) * 8192;
  u32x4 xq[4];
  {
    wait_ge(lds + W_LANDED, P_Q + 8 * (w + 1));
    STAMP(7);
    u32x4 qf[8];
    unsigned qa = slotQ + xr;
    asm volatile("" : "+v"(qa));
#pragma unroll
    for (int t = 0; t < 8; ++t) qf[t] = lds_rd128(qa ^ (t << 5));
    SCHED_FENCE();
    f32x16 acc[2] = {};
    float ss[4] = {};
#pragma unroll
    for (int t = 0; t < 8; ++t)
#pragma unroll
      for (int i = 0; i < 4; ++i) ss[i] = dot2(qf[t][i], qf[t][i], ss[i]);
#pragma unroll
    for (int t = 0; t < 8; ++t) acc[0] = MFMA(as_frag(om[0][t]), as_frag(qf[t]), acc[0]);
    const float bias = add_halves((ss[0] + ss[1]) + (ss[2] + ss[3])) * c2 - 3.0f;
#pragma unroll
    for (int t = 0; t < 8; ++t) acc[1] = MFMA(as_frag(om[1][t]), as_frag(qf[t]), acc[1]);
#pragma unroll
    for (int u = 0; u < 4; ++u)
#pragma unroll
      for (int j = 0; j < 4; ++j) {
        const int e = 8 * (u & 1) + 2 * j;
        xq[u][j] = pk_bf16(__builtin_amdgcn_exp2f(acc[u >> 1][e] * c1 + bias) + 1e-6f, __builtin_amdgcn_exp2f(acc[u >> 1][e + 1] * c1 + bias) + 1e-6f);
      }
    asm volatile("" : "+v"(xq[0]), "+v"(xq[1]), "+v"(xq[2]), "+v"(xq[3]));
    SCHED_FENCE();
  }
  unsigned wb = slotQ + r * 256 + 8 * (h ^ ((r >> 3) & 1)), wx = (r & 7) << 4;
  char* dst = out + (row0 + w * 32) * 256;
  unsigned so = (lane >> 3) * 256 + (lane & 7) * 16;
  unsigned rb = slotQ + (lane >> 3) * 256 + (((lane & 7) ^ (lane >> 3)) << 4);
  unsigned kb = lds + LDS_B + l16, zb = lds + LDS_Z + h * 16;
  asm volatile("" : "+v"(wb), "+v"(wx), "+v"(so), "+v"(rb), "+v"(kb), "+v"(zb), "+s"(dst));

  __builtin_amdgcn_s_setprio(1);
  wait_ge(lds + W_KV, 8);
  wait_ge(lds + W_LANDED, P_END - GATE);
  STAMP(6);
  {
    u32x4 pa[7], pb[7];
    u32x2 px[7];
    const unsigned xo = zwave ? lane * 8 : (lane >> 1) * 8;
    const bool hi = !zwave && (lane & 1);
#define REC_LOAD(i, TAIL, C)                                                                                          \
  asm volatile("global_load_dwordx4 %0, %3, %5 sc1\n\tglobal_load_dwordx4 %1, %3, %5 offset:1024 sc1\n\t"             \
               "global_load_dwordx2 %2, %4, %6 sc1" TAIL : C(pa[i]), C(pb[i]), C(px[i])                               \
               : "v"(l16), "v"(xo), "s"(kvbuf + ((chunk + 1 + (i)) & 7) * 2048), "s"(auxbuf + ((chunk + 1 + (i)) & 7) * 512) : "memory")
#define REC_BAD(i) (__builtin_amdgcn_ballot_w64(check_word(pa[i], pb[i], zwave ? px[i][1] : 0u, salt) != (hi ? px[i][1] : px[i][0])) != 0)
#pragma unroll
    for (int i = 0; i < 7; ++i) REC_LOAD(i, "", "=&v");
    if (w >= 4) __builtin_amdgcn_s_setprio(2);
    STAMP(14);
    const u32x4 id0 = lds_rd128(lds + LDS_ID + l16), id1 = lds_rd128(lds + LDS_ID + 1024 + l16);
    unsigned spin = 0;
#pragma unroll
    for (int i = 0; i < 7; ++i) {
      asm volatile("s_waitcnt vmcnt(%3)" : "+v"(pa[i]), "+v"(pb[i]), "+v"(px[i]) : "n"(18 - 3 * i) : "memory");
      if (__builtin_expect(REC_BAD(i), 0)) {
        do {
          COUNT_RETRY();
          __builtin_amdgcn_s_sleep(2);
          REC_LOAD(i, "\n\ts_waitcnt vmcnt(0)", "+v");
        } while (REC_BAD(i) && ++spin < SPIN_CAP);
      }
      kv = MFMA(as_frag(id0), as_frag(pa[i]), kv);
      kv = MFMA(as_frag(id1), as_frag(pb[i]), kv);
      const unsigned zu = px[i][1];
      zown += __builtin_bit_cast(float, zu);
    }
    STAMP(8);
    if (zwave && lane < 32)
      *(LAS unsigned short*)(size_t)(lds + LDS_Z + mt * 64 + (((r >> 4) * 2 + ((r >> 2) & 1)) * 8 + ((r >> 3) & 1) * 4 + (r & 3)) * 2) = (unsigned short)pk_bf16(zown, 0.f);
    u32x4 d0, d1;
    pack_tile(kv, d0, d1);
    lds_wr128(kb + (mt * 4 + dt) * 2048, d0);
    lds_wr128(kb + (mt * 4 + dt) * 2048 + 1024, d1);
    arrive(lds + W_H + (w >> 2) * 4);
  }
  STAMP(9);

  {
    float inv;
#pragma unroll
    for (int half = 0; half < 2; ++half) {
      wait_ge(lds + W_H + half * 4, 4);
      if (half) STAMP(3); else STAMP(10);
      u32x4 zf[4], kf2[2][4];
      if (half == 0) {
#pragma unroll
        for (int u = 0; u < 4; ++u) zf[u] = lds_rd128(zb + u * 32);
      }
#pragma unroll
      for (int d = 0; d < 2; ++d) {
        SCHED_FENCE();
#pragma unroll
        for (int u = 0; u < 4; ++u) kf2[d][u] = lds_rd128(kb + ((u >> 1) * 4 + 2 * half + d) * 2048 + (u & 1) * 1024);
      }
      SCHED_FENCE();
      f32x16 den = {}, o[2] = {};
      if (half == 0) {
#pragma unroll
        for (int u = 0; u < 4; ++u) den = MFMA(as_frag(zf[u]), as_frag(xq[u]), den);
      }
#pragma unroll
      for (int d = 0; d < 2; ++d)
#pragma unroll
        for (int u = 0; u < 4; ++u) o[d] = MFMA(as_frag(kf2[d][u]), as_frag(xq[u]), o[d]);
      if (half == 0) inv = __builtin_amdgcn_rcpf(den[0] + 1e-6f);
#pragma unroll
      for (int d = 0; d < 2; ++d)
#pragma unroll
        for (int g = 0; g < 4; ++g) {
          const u32x2 pk = {pk_bf16(o[d][4 * g] * inv, o[d][4 * g + 1] * inv), pk_bf16(o[d][4 * g + 2] * inv, o[d][4 * g + 3] * inv)};
          lds_wr64(wb + (((8 * half + 4 * d + g) << 4) ^ wx), pk);
        }
      u32x4 x[4];
#pragma unroll
      for (int i = 0; i < 4; ++i) x[i] = lds_rd128(rb + i * 2048 + half * 128);
#pragma unroll
      for (int i = 0; i < 4; ++i) {
        if (i & 1) x[i] = (u32x4){x[i][2], x[i][3], x[i][0], x[i][1]};
        ST128_DEV(dst, so + (i >> 1) * 4096, x[i], (i & 1) * 2048 + half * 128);
      }
      if (half == 0) STAMP(2);
    }
  }
  if (chunk == 0 && w == 0 && lane == 0)
    asm volatile("global_store_dword %0, %1, %2 sc1" ::"v"(0u), "v"(tag), "s"(epw) : "memory");
  STAMP(11);
  asm volatile(".Lrfa_end_b:");
}

extern "C" void kernel_launch(void* const* d_in, const int* in_sizes, int n_in, void* d_out, int out_size, void* d_ws,
                              size_t ws_size, hipStream_t stream) {
  rfa_kernel<<<256, 576, 0, stream>>>((const char*)d_in[0], (const char*)d_in[1], (const char*)d_in[2], (const char*)d_in[3],
                                      (char*)d_out, (char*)d_ws);
}
```

```cpp
#include <hip/hip_runtime.h>
#include <hip/hip_bf16.h>
#include <stdint.h>

typedef __bf16 bf16x8 __attribute__((ext_vector_type(8)));
typedef __bf16 bf16x2 __attribute__((ext_vector_type(2)));
typedef short s16x4 __attribute__((ext_vector_type(4)));
typedef float f32x16 __attribute__((ext_vector_type(16)));
typedef float f32x4 __attribute__((ext_vector_type(4)));
typedef unsigned u32x4 __attribute__((ext_vector_type(4)));
typedef unsigned u32x2 __attribute__((ext_vector_type(2)));
#define LAS __attribute__((address_space(3)))
#define DI __device__ __forceinline__

#define LD_POL "nt"
constexpr int WIN = 24;
constexpr int PRO = 40;
constexpr int GATE = 16;

constexpr int S = 2048;
constexpr int LDS_A = 0;
constexpr int LDS_B = 65536;
constexpr int LDS_OM = LDS_B + 49152;
constexpr int LDS_C = 131072;
constexpr int LDS_D = 155648;
constexpr int W_LANDED = LDS_D, W_OM = LDS_D + 4, W_PHI = LDS_D + 8, W_AF = LDS_D + 12, W_KV = LDS_D + 16, W_H = LDS_D + 20;
constexpr int LDS_Z = LDS_D + 128, LDS_ID = LDS_D + 256;
constexpr int P_K = 16, P_V = 80, P_Q = 144, P_END = 208;
constexpr size_t WS_EP = 0;
constexpr size_t WS_AUX = 65536;
constexpr size_t WS_KV = WS_AUX + 1048576;
constexpr unsigned SPIN_CAP = 1u << 18;

#define MFMA(a, b, c) __builtin_amdgcn_mfma_f32_32x32x16_bf16((a), (b), (c), 0, 0, 0)
#define WAIT_VM(n) asm volatile("s_waitcnt vmcnt(%0)" ::"n"(n) : "memory")
#define SCHED_FENCE() __builtin_amdgcn_sched_barrier(0)

DI unsigned pk_bf16(float lo, float hi) { bf16x2 p; p[0] = (__bf16)lo; p[1] = (__bf16)hi; return __builtin_bit_cast(unsigned, p); }
DI bf16x8 as_frag(u32x4 v) { return __builtin_bit_cast(bf16x8, v); }
DI u32x4 lds_rd128(unsigned a) { return *(const LAS u32x4*)(size_t)a; }
DI void lds_wr128(unsigned a, u32x4 v) { *(LAS u32x4*)(size_t)a = v; }
DI void lds_wr64(unsigned a, u32x2 v) { *(LAS u32x2*)(size_t)a = v; }
DI float dot2(unsigned a, unsigned b, float c) { return __builtin_amdgcn_fdot2_f32_bf16(__builtin_bit_cast(bf16x2, a), __builtin_bit_cast(bf16x2, b), c, false); }
DI float add_halves(float x) {
  const unsigned u = __builtin_bit_cast(unsigned, x);
  const auto r = __builtin_amdgcn_permlane32_swap(u, u, false, false);
  return __builtin_bit_cast(float, (unsigned)r[0]) + __builtin_bit_cast(float, (unsigned)r[1]);
}
DI void pack_tile(const f32x16& t, u32x4& d0, u32x4& d1) {
#pragma unroll
  for (int j = 0; j < 4; ++j) { d0[j] = pk_bf16(t[2 * j], t[2 * j + 1]); d1[j] = pk_bf16(t[8 + 2 * j], t[8 + 2 * j + 1]); }
}
DI unsigned rot(unsigned x, int k) { return __builtin_amdgcn_alignbit(x, x, k); }
DI unsigned check_word(u32x4 a, u32x4 b, unsigned z, unsigned salt) {
  return ((a[0] + rot(a[1], 3) + rot(a[2], 5)) + (rot(a[3], 7) + rot(b[0], 9) + rot(b[1], 11))) + ((rot(b[2], 13) + rot(b[3], 15) + z) + salt);
}

#define DMA_QUAD(lds, g, v0, v1, v2, v3, POL)                                                   \
  asm volatile("s_mov_b32 m0, %0\n\ts_nop 0\n\t"                                                \
               "global_load_lds_dwordx4 %2, %1 offset:0 " POL "\n\t"                            \
               "global_load_lds_dwordx4 %3, %1 offset:1024 " POL "\n\t"                         \
               "global_load_lds_dwordx4 %4, %1 offset:2048 " POL "\n\t"                         \
               "global_load_lds_dwordx4 %5, %1 offset:3072 " POL                                \
               ::"s"(lds), "s"(g), "v"(v0), "v"(v1), "v"(v2), "v"(v3) : "memory")
#define ST128_DEV(g, voff, val, off)                                                            \
  asm volatile("global_store_dwordx4 %0, %1, %2 offset:%3 sc1\n\ts_nop 1" ::"v"(voff), "v"(val), "s"(g), "n"(off) : "memory")

DI void wait_ge(unsigned addr, unsigned need) {
  for (;;) {
    unsigned x;
    asm volatile("ds_read_b32 %0, %1\n\ts_waitcnt lgkmcnt(0)" : "=v"(x) : "v"(addr) : "memory");
    if ((unsigned)__builtin_amdgcn_readfirstlane(x) >= need) break;
    __builtin_amdgcn_s_sleep(1);
  }
}
DI void arrive(unsigned addr) {
  unsigned long long ex;
  asm volatile("s_mov_b64 %0, exec\n\ts_mov_b64 exec, 1\n\tds_add_u32 %1, %2\n\ts_mov_b64 exec, %0" : "=&s"(ex) : "v"(addr), "v"(1u) : "memory");
}
DI void post(unsigned addr, unsigned val) { asm volatile("ds_write_b32 %0, %1" ::"v"(addr), "v"(val) : "memory"); }

__global__ __launch_bounds__(576, 3) void rfa_kernel(const char* __restrict__ q, const char* __restrict__ k,
                                                     const char* __restrict__ v, const char* __restrict__ omega,
                                                     char* __restrict__ out, char* ws) {
  __shared__ __attribute__((aligned(16))) char smem[163840];
  unsigned long long pc0;
  int left_a, left_b;
  asm volatile("s_getpc_b64 %0\n.Lrfa_pc:\n\ts_mov_b32 %1, .Lrfa_end_a-.Lrfa_pc\n\ts_mov_b32 %2, .Lrfa_end_b-.Lrfa_pc" : "=s"(pc0), "=s"(left_a), "=s"(left_b));
  const unsigned lds = (unsigned)(size_t)(LAS char*)smem;
  const int w = __builtin_amdgcn_readfirstlane(threadIdx.x >> 6);
  const unsigned lane = threadIdx.x & 63, r = lane & 31, h = lane >> 5;
  const int b = blockIdx.x, head = (b & 7) * 4 + (b >> 6), chunk = (b >> 3) & 7;
  const size_t row0 = (size_t)head * S + chunk * 256;
  const unsigned l16 = lane * 16;
  unsigned vs[4];
#pragma unroll
  for (int i = 0; i < 4; ++i) vs[i] = (lane >> 4) * 256 + (((lane & 15) ^ ((4 * i + (lane >> 4)) & 15)) << 4);
  const char* kk = k + row0 * 256;
  if (w == 8) {
#pragma unroll 1
    for (int j = 0; j < 4; ++j) DMA_QUAD(lds + LDS_OM + j * 4096, omega + j * 4096, vs[0], vs[1], vs[2], vs[3], "");
#pragma unroll 1
    for (int j = 0; j < (PRO - P_K) / 4; ++j) DMA_QUAD(lds + LDS_A + j * 4096, kk + j * 4096, vs[0], vs[1], vs[2], vs[3], LD_POL);
    post(lds + LDS_D + (lane & 7) * 4, 0u);
  }
  if (w == 7) {
    const unsigned pos = ((r >> 2) & 1) == h ? ((r >> 4) << 2) | (((r >> 3) & 1) << 1) | ((r >> 1) & 1) : 8u;
    const unsigned one = (r & 1) ? 0x3F800000u : 0x3F80u;
    u32x4 id0, id1;
#pragma unroll
    for (int j = 0; j < 4; ++j) { id0[j] = pos == j ? one : 0u; id1[j] = pos == 4 + j ? one : 0u; }
    lds_wr128(lds + LDS_ID + l16, id0);
    lds_wr128(lds + LDS_ID + 1024 + l16, id1);
  }
  asm volatile("s_waitcnt lgkmcnt(0)\n\ts_barrier" ::: "memory");

  if (w == 8) {
    const unsigned vv = (lane >> 4) * 256 + (((lane & 15) ^ ((lane >> 4) << 2)) << 4);
    const char* vsrc = v + row0 * 256;
    const char* qq = q + row0 * 256;
#pragma unroll
    for (int n = PRO - 16; n > WIN - 4; n -= 4) {
      WAIT_VM(n);
      post(lds + W_LANDED, PRO - n);
    }
    unsigned done = PRO - WIN;
    auto run = [&](int quads, unsigned dst, const char* src, unsigned v0, unsigned v1, unsigned v2, unsigned v3) {
#pragma unroll 1
      for (int j = 0; j < quads; ++j) {
        DMA_QUAD(dst, src, v0, v1, v2, v3, LD_POL);
        dst += 4096; src += 4096; done += 4;
        WAIT_VM(WIN);
        post(lds + W_LANDED, done);
      }
    };
    run((P_V - PRO) / 4, lds + LDS_A + (PRO - P_K) * 1024, kk + (PRO - P_K) * 1024, vs[0], vs[1], vs[2], vs[3]);
    run(12, lds + LDS_B, vsrc, vv, vv, vv, vv);
    wait_ge(lds + W_OM, 8);
    run(4, lds + LDS_B + 49152, vsrc + 49152, vv, vv, vv, vv);
    run(6, lds + LDS_C, qq, vs[0], vs[1], vs[2], vs[3]);
    wait_ge(lds + W_AF, 8);
    run(10, lds + LDS_A, qq + 24576, vs[0], vs[1], vs[2], vs[3]);
#pragma unroll
    for (int n = WIN - 4; n >= 0; n -= 4) {
      WAIT_VM(n);
      post(lds + W_LANDED, P_END - n);
    }
    asm volatile(".Lrfa_end_a:");
    return;
  }

  const int mt = (w >> 1) & 1, dt = (w & 1) | ((w >> 2) << 1);
  const bool zwave = dt == 0;
  char* epw = ws + WS_EP + head * 128;
  char* auxbuf = ws + WS_AUX + (size_t)(head * 8 + w) * 4096;
  char* kvbuf = ws + WS_KV + (size_t)(head * 8 + w) * 16384;
  __builtin_amdgcn_s_setprio(1);
  unsigned ep;
  asm volatile("global_load_dword %0, %1, %2 sc1" : "=v"(ep) : "v"(0u), "s"(epw) : "memory");
  unsigned warm, warm2;
  asm volatile("s_load_dword %0, %2, 0x0\n\ts_load_dword %1, %2, 0x40" : "=&s"(warm), "=&s"(warm2) : "s"(pc0 + min((b >> 3) * 8 + w, (max(left_a, left_b) >> 7) - 1) * 128) : "memory");

  const unsigned xr = r * 256 + ((h ^ (r & 15)) << 4);
  u32x4 om[2][8];
  wait_ge(lds + W_LANDED, P_K);
  asm volatile("" ::"s"(warm), "s"(warm2));
#pragma unroll
  for (int i = 0; i < 2; ++i)
#pragma unroll
    for (int t = 0; t < 8; ++t) om[i][t] = lds_rd128(((lds + LDS_OM + xr) ^ (t << 5)) + i * 8192);
  arrive(lds + W_OM);

  const float scale = 0.29730177875068026f;
  const float c1 = scale * 1.4426950408889634f;
  const float c2 = -0.5f * scale * scale * 1.4426950408889634f;

  {
    const unsigned slotA = lds + LDS_A + w * 8192;
    wait_ge(lds + W_LANDED, P_K + 8 * (w + 1));
    u32x4 kf[8];
    unsigned ka = slotA + xr;
    asm volatile("" : "+v"(ka));
#pragma unroll
    for (int t = 0; t < 8; ++t) kf[t] = lds_rd128(ka ^ (t << 5));
    SCHED_FENCE();
    f32x16 acc[2] = {};
    float ss[4] = {};
#pragma unroll
    for (int t = 0; t < 8; ++t)
#pragma unroll
      for (int i = 0; i < 4; ++i) ss[i] = dot2(kf[t][i], kf[t][i], ss[i]);
#pragma unroll
    for (int t = 0; t < 8; ++t) acc[0] = MFMA(as_frag(kf[t]), as_frag(om[0][t]), acc[0]);
    const float bias = add_halves((ss[0] + ss[1]) + (ss[2] + ss[3])) * c2 - 3.0f;
    *(LAS float*)(size_t)(slotA + 4096 + r * 4) = bias;
    f32x4 bz[4];
#pragma unroll
    for (int g = 0; g < 4; ++g) bz[g] = *(const LAS f32x4*)(size_t)(slotA + 4096 + (8 * g + 4 * h) * 4);
#pragma unroll
    for (int t = 0; t < 8; ++t) acc[1] = MFMA(as_frag(kf[t]), as_frag(om[1][t]), acc[1]);
#pragma unroll
    for (int i = 0; i < 2; ++i)
#pragma unroll
      for (int s = 0; s < 2; ++s) {
        u32x4 f;
#pragma unroll
        for (int j = 0; j < 4; ++j) {
          const int e = 8 * s + 2 * j;
          const float p0 = __builtin_amdgcn_exp2f(acc[i][e] * c1 + bz[e >> 2][e & 3]) + 1e-6f;
          const float p1 = __builtin_amdgcn_exp2f(acc[i][e + 1] * c1 + bz[(e + 1) >> 2][(e + 1) & 3]) + 1e-6f;
          f[j] = pk_bf16(p0, p1);
        }
        lds_wr128(slotA + (i * 2 + s) * 1024 + l16, f);
      }
  }
  arrive(lds + W_PHI);
  asm volatile("s_waitcnt vmcnt(0)" : "+v"(ep)::"memory");
  const unsigned tag = __builtin_amdgcn_readfirstlane(ep) + 1;
  const unsigned salt = tag + lane * 0x9E3779B1u;
  wait_ge(lds + W_PHI, 8);

  f32x16 kv = {};
  float zown = 0.f;
  {
    const unsigned ab = lds + LDS_A + mt * 2048 + l16;
    u32x4 af[16];
#pragma unroll
    for (int ks = 0; ks < 10; ++ks) af[ks] = lds_rd128(ab + (ks >> 1) * 8192 + (ks & 1) * 1024);
    arrive(lds + W_AF);
    float zs[4] = {};
    const unsigned qq = (lane & 15) >> 2, p = lane & 3, blk = (lane >> 4) & 1;
    const unsigned trb = lds + LDS_B + (4 * h + qq) * 256 + (((4 * dt + 2 * blk + (p >> 1)) ^ (qq << 2)) << 4) + 8 * (p & 1);
#pragma unroll
    for (int c = 0; c < 4; ++c) {
      if (c == 1 || c == 2) {
#pragma unroll
        for (int ks = (c == 1 ? 10 : 12); ks < (c == 1 ? 12 : 16); ++ks) af[ks] = lds_rd128(ab + (ks >> 1) * 8192 + (ks & 1) * 1024);
      }
      if (zwave && c < 3) {
#pragma unroll
        for (int ks = (c == 0 ? 0 : 4 * c + 4); ks < 4 * c + 8; ++ks)
#pragma unroll
          for (int i = 0; i < 4; ++i) zs[i] = dot2(af[ks][i], 0x3F803F80u, zs[i]);
        if (c == 2) zown = add_halves((zs[0] + zs[1]) + (zs[2] + zs[3]));
      }
      SCHED_FENCE();
      wait_ge(lds + W_LANDED, P_V + 16 * (c + 1));
      s16x4 vf[8];
#pragma unroll
      for (int u = 0; u < 8; ++u) vf[u] = __builtin_amdgcn_ds_read_tr16_b64_v4i16((LAS s16x4*)(size_t)(trb + (64 * c + 8 * u) * 256));
      SCHED_FENCE();
#pragma unroll
      for (int u = 0; u < 4; ++u)
        kv = MFMA(as_frag(af[4 * c + u]), __builtin_bit_cast(bf16x8, __builtin_shufflevector(vf[2 * u], vf[2 * u + 1], 0, 1, 2, 3, 4, 5, 6, 7)), kv);
    }
    arrive(lds + W_KV);
  }
  {
    u32x4 d0, d1;
    pack_tile(kv, d0, d1);
    ST128_DEV(kvbuf + chunk * 2048, l16, d0, 0);
    ST128_DEV(kvbuf + chunk * 2048, l16, d1, 1024);
    const unsigned zb = __builtin_bit_cast(unsigned, zown);
    const u32x2 ax = {check_word(d0, d1, zb, salt), zb};
    if (zwave) asm volatile("global_store_dwordx2 %0, %1, %2 sc1" ::"v"(lane * 8), "v"(ax), "s"(auxbuf + chunk * 512) : "memory");
    else asm volatile("global_store_dword %0, %1, %2 sc1" ::"v"(lane * 4), "v"(ax[0]), "s"(auxbuf + chunk * 512) : "memory");
  }

  if (w >= 4) __builtin_amdgcn_s_setprio(2);
  const unsigned slotQ = w < 3 ? lds + LDS_C + w * 8192 : lds + LDS_A + (w - 3) * 8192;
  u32x4 xq[4];
  {
    wait_ge(lds + W_LANDED, P_Q + 8 * (w + 1));
    u32x4 qf[8];
    unsigned qa = slotQ + xr;
    asm volatile("" : "+v"(qa));
#pragma unroll
    for (int t = 0; t < 8; ++t) qf[t] = lds_rd128(qa ^ (t << 5));
    SCHED_FENCE();
    f32x16 acc[2] = {};
    float ss[4] = {};
#pragma unroll
    for (int t = 0; t < 8; ++t)
#pragma unroll
      for (int i = 0; i < 4; ++i) ss[i] = dot2(qf[t][i], qf[t][i], ss[i]);
#pragma unroll
    for (int t = 0; t < 8; ++t) acc[0] = MFMA(as_frag(om[0][t]), as_frag(qf[t]), acc[0]);
    const float bias = add_halves((ss[0] + ss[1]) + (ss[2] + ss[3])) * c2 - 3.0f;
#pragma unroll
    for (int t = 0; t < 8; ++t) acc[1] = MFMA(as_frag(om[1][t]), as_frag(qf[t]), acc[1]);
#pragma unroll
    for (int u = 0; u < 4; ++u)
#pragma unroll
      for (int j = 0; j < 4; ++j) {
        const int e = 8 * (u & 1) + 2 * j;
        xq[u][j] = pk_bf16(__builtin_amdgcn_exp2f(acc[u >> 1][e] * c1 + bias) + 1e-6f, __builtin_amdgcn_exp2f(acc[u >> 1][e + 1] * c1 + bias) + 1e-6f);
      }
    asm volatile("" : "+v"(xq[0]), "+v"(xq[1]), "+v"(xq[2]), "+v"(xq[3]));
    SCHED_FENCE();
  }
  unsigned wb = slotQ + r * 256 + 8 * (h ^ ((r >> 3) & 1)), wx = (r & 7) << 4;
  char* dst = out + (row0 + w * 32) * 256;
  unsigned so = (lane >> 3) * 256 + (lane & 7) * 16;
  unsigned rb = slotQ + (lane >> 3) * 256 + (((lane & 7) ^ (lane >> 3)) << 4);
  unsigned kb = lds + LDS_B + l16, zb = lds + LDS_Z + h * 16;
  asm volatile("" : "+v"(wb), "+v"(wx), "+v"(so), "+v"(rb), "+v"(kb), "+v"(zb), "+s"(dst));

  __builtin_amdgcn_s_setprio(1);
  wait_ge(lds + W_KV, 8);
  wait_ge(lds + W_LANDED, P_END - GATE);
  {
    u32x4 pa[7], pb[7];
    u32x2 px[7];
    const unsigned xo = zwave ? lane * 8 : (lane >> 1) * 8;
    const bool hi = !zwave && (lane & 1);
#define REC_LOAD(i, TAIL, C)                                                                                          \
  asm volatile("global_load_dwordx4 %0, %3, %5 sc1\n\tglobal_load_dwordx4 %1, %3, %5 offset:1024 sc1\n\t"             \
               "global_load_dwordx2 %2, %4, %6 sc1" TAIL : C(pa[i]), C(pb[i]), C(px[i])                               \
               : "v"(l16), "v"(xo), "s"(kvbuf + ((chunk + 1 + (i)) & 7) * 2048), "s"(auxbuf + ((chunk + 1 + (i)) & 7) * 512) : "memory")
#define REC_BAD(i) (__builtin_amdgcn_ballot_w64(check_word(pa[i], pb[i], zwave ? px[i][1] : 0u, salt) != (hi ? px[i][1] : px[i][0])) != 0)
#pragma unroll
    for (int i = 0; i < 7; ++i) REC_LOAD(i, "", "=&v");
    if (w >= 4) __builtin_amdgcn_s_setprio(2);
    const u32x4 id0 = lds_rd128(lds + LDS_ID + l16), id1 = lds_rd128(lds + LDS_ID + 1024 + l16);
    unsigned spin = 0;
#pragma unroll
    for (int i = 0; i < 7; ++i) {
      asm volatile("s_waitcnt vmcnt(%3)" : "+v"(pa[i]), "+v"(pb[i]), "+v"(px[i]) : "n"(18 - 3 * i) : "memory");
      if (__builtin_expect(REC_BAD(i), 0)) {
        do {
          __builtin_amdgcn_s_sleep(2);
          REC_LOAD(i, "\n\ts_waitcnt vmcnt(0)", "+v");
        } while (REC_BAD(i) && ++spin < SPIN_CAP);
      }
      kv = MFMA(as_frag(id0), as_frag(pa[i]), kv);
      kv = MFMA(as_frag(id1), as_frag(pb[i]), kv);
      const unsigned zu = px[i][1];
      zown += __builtin_bit_cast(float, zu);
    }
    if (zwave && lane < 32)
      *(LAS unsigned short*)(size_t)(lds + LDS_Z + mt * 64 + (((r >> 4) * 2 + ((r >> 2) & 1)) * 8 + ((r >> 3) & 1) * 4 + (r & 3)) * 2) = (unsigned short)pk_bf16(zown, 0.f);
    u32x4 d0, d1;
    pack_tile(kv, d0, d1);
    lds_wr128(kb + (mt * 4 + dt) * 2048, d0);
    lds_wr128(kb + (mt * 4 + dt) * 2048 + 1024, d1);
    arrive(lds + W_H + (w >> 2) * 4);
  }

  {
    float inv;
#pragma unroll
    for (int half = 0; half < 2; ++half) {
      wait_ge(lds + W_H + half * 4, 4);
      u32x4 zf[4], kf2[2][4];
      if (half == 0) {
#pragma unroll
        for (int u = 0; u < 4; ++u) zf[u] = lds_rd128(zb + u * 32);
      }
#pragma unroll
      for (int d = 0; d < 2; ++d) {
        SCHED_FENCE();
#pragma unroll
        for (int u = 0; u < 4; ++u) kf2[d][u] = lds_rd128(kb + ((u >> 1) * 4 + 2 * half + d) * 2048 + (u & 1) * 1024);
      }
      SCHED_FENCE();
      f32x16 den = {}, o[2] = {};
      if (half == 0) {
#pragma unroll
        for (int u = 0; u < 4; ++u) den = MFMA(as_frag(zf[u]), as_frag(xq[u]), den);
      }
#pragma unroll
      for (int d = 0; d < 2; ++d)
#pragma unroll
        for (int u = 0; u < 4; ++u) o[d] = MFMA(as_frag(kf2[d][u]), as_frag(xq[u]), o[d]);
      if (half == 0) inv = __builtin_amdgcn_rcpf(den[0] + 1e-6f);
#pragma unroll
      for (int d = 0; d < 2; ++d)
#pragma unroll
        for (int g = 0; g < 4; ++g) {
          const u32x2 pk = {pk_bf16(o[d][4 * g] * inv, o[d][4 * g + 1] * inv), pk_bf16(o[d][4 * g + 2] * inv, o[d][4 * g + 3] * inv)};
          lds_wr64(wb + (((8 * half + 4 * d + g) << 4) ^ wx), pk);
        }
      u32x4 x[4];
#pragma unroll
      for (int i = 0; i < 4; ++i) x[i] = lds_rd128(rb + i * 2048 + half * 128);
#pragma unroll
      for (int i = 0; i < 4; ++i) {
        if (i & 1) x[i] = (u32x4){x[i][2], x[i][3], x[i][0], x[i][1]};
        ST128_DEV(dst, so + (i >> 1) * 4096, x[i], (i & 1) * 2048 + half * 128);
      }
    }
  }
  if (chunk == 0 && w == 0 && lane == 0)
    asm volatile("global_store_dword %0, %1, %2 sc1" ::"v"(0u), "v"(tag), "s"(epw) : "memory");
  asm volatile(".Lrfa_end_b:");
}

extern "C" void kernel_launch(void* const* d_in, const int* in_sizes, int n_in, void* d_out, int out_size, void* d_ws,
                              size_t ws_size, hipStream_t stream) {
  rfa_kernel<<<256, 576, 0, stream>>>((const char*)d_in[0], (const char*)d_in[1], (const char*)d_in[2], (const char*)d_in[3],
                                      (char*)d_out, (char*)d_ws);
}
```

```cpp
#include <hip/hip_runtime.h>
#include <hip/hip_bf16.h>
#include <stdint.h>

typedef __bf16 bf16x8 __attribute__((ext_vector_type(8)));
typedef __bf16 bf16x2 __attribute__((ext_vector_type(2)));
typedef short s16x4 __attribute__((ext_vector_type(4)));
typedef float f32x16 __attribute__((ext_vector_type(16)));
typedef float f32x4 __attribute__((ext_vector_type(4)));
typedef unsigned u32x4 __attribute__((ext_vector_type(4)));
typedef unsigned u32x2 __attribute__((ext_vector_type(2)));
#define LAS __attribute__((address_space(3)))
#define DI __device__ __forceinline__

#define LD_POL "nt"
#define WIN 24
#define PRO 40
#define GATE 16
#define ROT 1

constexpr int S = 2048;
constexpr int LDS_A = 0;
constexpr int LDS_B = 65536;
constexpr int LDS_OM = LDS_B + 49152;
constexpr int LDS_C = 131072;
constexpr int LDS_D = 155648;
constexpr int W_LANDED = LDS_D, W_OM = LDS_D + 4, W_PHI = LDS_D + 8, W_AF = LDS_D + 12, W_KV = LDS_D + 16, W_H = LDS_D + 20;
constexpr int LDS_Z = LDS_D + 128, LDS_ID = LDS_D + 256;
constexpr int P_K = 16, P_V = 80, P_Q = 144, P_END = 208;
constexpr size_t WS_EP = 0;
constexpr size_t WS_AUX = 65536;
constexpr size_t WS_KV = WS_AUX + 1048576;
constexpr unsigned SPIN_CAP = 1u << 18;

#define STAMP(i)
#define COUNT_RETRY()
#define MFMA(a, b, c) __builtin_amdgcn_mfma_f32_32x32x16_bf16((a), (b), (c), 0, 0, 0)
#define WAIT_VM(n) asm volatile("s_waitcnt vmcnt(%0)" ::"n"(n) : "memory")
#define SCHED_FENCE() __builtin_amdgcn_sched_barrier(0)

DI unsigned pk_bf16(float lo, float hi) { bf16x2 p; p[0] = (__bf16)lo; p[1] = (__bf16)hi; return __builtin_bit_cast(unsigned, p); }
DI bf16x8 as_frag(u32x4 v) { return __builtin_bit_cast(bf16x8, v); }
DI u32x4 lds_rd128(unsigned a) { return *(const LAS u32x4*)(size_t)a; }
DI void lds_wr128(unsigned a, u32x4 v) { *(LAS u32x4*)(size_t)a = v; }
DI void lds_wr64(unsigned a, u32x2 v) { *(LAS u32x2*)(size_t)a = v; }
DI float dot2(unsigned a, unsigned b, float c) { return __builtin_amdgcn_fdot2_f32_bf16(__builtin_bit_cast(bf16x2, a), __builtin_bit_cast(bf16x2, b), c, false); }
DI float add_halves(float x) {
  const unsigned u = __builtin_bit_cast(unsigned, x);
  const auto r = __builtin_amdgcn_permlane32_swap(u, u, false, false);
  return __builtin_bit_cast(float, (unsigned)r[0]) + __builtin_bit_cast(float, (unsigned)r[1]);
}
DI void pack_tile(const f32x16& t, u32x4& d0, u32x4& d1) {
#pragma unroll
  for (int j = 0; j < 4; ++j) { d0[j] = pk_bf16(t[2 * j], t[2 * j + 1]); d1[j] = pk_bf16(t[8 + 2 * j], t[8 + 2 * j + 1]); }
}
DI unsigned rot(unsigned x, int k) { return ROT ? __builtin_amdgcn_alignbit(x, x, k) : x; }
DI unsigned check_word(u32x4 a, u32x4 b, unsigned z, unsigned salt) {
  return ((a[0] + rot(a[1], 3) + rot(a[2], 5)) + (rot(a[3], 7) + rot(b[0], 9) + rot(b[1], 11))) + ((rot(b[2], 13) + rot(b[3], 15) + z) + salt);
}

#define DMA_QUAD(lds, g, v0, v1, v2, v3, POL)                                                   \
  asm volatile("s_mov_b32 m0, %0\n\ts_nop 0\n\t"                                                \
               "global_load_lds_dwordx4 %2, %1 offset:0 " POL "\n\t"                            \
               "global_load_lds_dwordx4 %3, %1 offset:1024 " POL "\n\t"                         \
               "global_load_lds_dwordx4 %4, %1 offset:2048 " POL "\n\t"                         \
               "global_load_lds_dwordx4 %5, %1 offset:3072 " POL                                \
               ::"s"(lds), "s"(g), "v"(v0), "v"(v1), "v"(v2), "v"(v3) : "memory")
#define ST128_DEV(g, voff, val, off)                                                            \
  asm volatile("global_store_dwordx4 %0, %1, %2 offset:%3 sc1\n\ts_nop 1" ::"v"(voff), "v"(val), "s"(g), "n"(off) : "memory")

DI void wait_ge(unsigned addr, unsigned need) {
  for (;;) {
    unsigned x;
    asm volatile("ds_read_b32 %0, %1\n\ts_waitcnt lgkmcnt(0)" : "=v"(x) : "v"(addr) : "memory");
    if ((unsigned)__builtin_amdgcn_readfirstlane(x) >= need) break;
    __builtin_amdgcn_s_sleep(1);
  }
}
DI void arrive(unsigned addr) {
  unsigned long long ex;
  asm volatile("s_mov_b64 %0, exec\n\ts_mov_b64 exec, 1\n\tds_add_u32 %1, %2\n\ts_mov_b64 exec, %0" : "=&s"(ex) : "v"(addr), "v"(1u) : "memory");
}
DI void post(unsigned addr, unsigned val) { asm volatile("ds_write_b32 %0, %1" ::"v"(addr), "v"(val) : "memory"); }

__global__ __launch_bounds__(576, 3) void rfa_kernel(const char* __restrict__ q, const char* __restrict__ k,
                                                     const char* __restrict__ v, const char* __restrict__ omega,
                                                     char* __restrict__ out, char* ws) {
  __shared__ __attribute__((aligned(16))) char smem[163840];
  STAMP(0);
  const unsigned lds = (unsigned)(size_t)(LAS char*)smem;
  const int w = __builtin_amdgcn_readfirstlane(threadIdx.x >> 6);
  const unsigned lane = threadIdx.x & 63;
  const unsigned vs0 = (lane >> 4) * 256 + (((lane & 15) ^ (lane >> 4)) << 4), vs1 = vs0 ^ 64, vs2 = vs0 ^ 128, vs3 = vs0 ^ 192;
  if (w == 8) {
#pragma unroll 1
    for (int j = 0; j < 4; ++j) DMA_QUAD(lds + LDS_OM + j * 4096, omega + j * 4096, vs0, vs1, vs2, vs3, "");
  }
  const int b = blockIdx.x, head = (b & 7) * 4 + (b >> 6), chunk = (b >> 3) & 7;
  const size_t row0 = (size_t)head * S + chunk * 256;
  const char* kk = k + row0 * 256;
  if (w == 8) {
#pragma unroll 1
    for (int j = 0; j < (PRO - P_K) / 4; ++j) DMA_QUAD(lds + LDS_A + j * 4096, kk + j * 4096, vs0, vs1, vs2, vs3, LD_POL);
    post(lds + LDS_D + (lane & 7) * 4, 0u);
  }
  const unsigned r = lane & 31, h = lane >> 5, l16 = lane * 16;
  unsigned long long pc0;
  int left_a, left_b;
  asm volatile("s_getpc_b64 %0\n.Lrfa_pc:\n\ts_mov_b32 %1, .Lrfa_end_a-.Lrfa_pc\n\ts_mov_b32 %2, .Lrfa_end_b-.Lrfa_pc" : "=s"(pc0), "=s"(left_a), "=s"(left_b));
  if (w == 7) {
    const unsigned pos = ((r >> 2) & 1) == h ? ((r >> 4) << 2) | (((r >> 3) & 1) << 1) | ((r >> 1) & 1) : 8u;
    const unsigned one = (r & 1) ? 0x3F800000u : 0x3F80u;
    u32x4 id0, id1;
#pragma unroll
    for (int j = 0; j < 4; ++j) { id0[j] = pos == j ? one : 0u; id1[j] = pos == 4 + j ? one : 0u; }
    lds_wr128(lds + LDS_ID + l16, id0);
    lds_wr128(lds + LDS_ID + 1024 + l16, id1);
  }
  asm volatile("s_waitcnt lgkmcnt(0)\n\ts_barrier" ::: "memory");

  if (w == 8) {
    const unsigned vv = (lane >> 4) * 256 + (((lane & 15) ^ ((lane >> 4) << 2)) << 4);
    const char* vsrc = v + row0 * 256;
    const char* qq = q + row0 * 256;
#pragma unroll
    for (int n = PRO - 16; n > WIN - 4; n -= 4) {
      WAIT_VM(n);
      post(lds + W_LANDED, PRO - n);
    }
    unsigned done = PRO - WIN;
    auto run = [&](int quads, unsigned dst, const char* src, unsigned v0, unsigned v1, unsigned v2, unsigned v3) {
#pragma unroll 1
      for (int j = 0; j < quads; ++j) {
        DMA_QUAD(dst, src, v0, v1, v2, v3, LD_POL);
        dst += 4096; src += 4096; done += 4;
        WAIT_VM(WIN);
        post(lds + W_LANDED, done);
      }
    };
    run((P_V - PRO) / 4, lds + LDS_A + (PRO - P_K) * 1024, kk + (PRO - P_K) * 1024, vs0, vs1, vs2, vs3);
    STAMP(1);
    run(12, lds + LDS_B, vsrc, vv, vv, vv, vv);
    wait_ge(lds + W_OM, 8);
    run(4, lds + LDS_B + 49152, vsrc + 49152, vv, vv, vv, vv);
    STAMP(2);
    run(6, lds + LDS_C, qq, vs0, vs1, vs2, vs3);
    wait_ge(lds + W_AF, 8);
    run(10, lds + LDS_A, qq + 24576, vs0, vs1, vs2, vs3);
    STAMP(3);
#pragma unroll
    for (int n = WIN - 4; n >= 0; n -= 4) {
      WAIT_VM(n);
      post(lds + W_LANDED, P_END - n);
    }
    STAMP(4);
    asm volatile(".Lrfa_end_a:");
    return;
  }

  const int mt = (w >> 1) & 1, dt = (w & 1) | ((w >> 2) << 1);
  const bool zwave = dt == 0;
  char* epw = ws + WS_EP + head * 128;
  char* auxbuf = ws + WS_AUX + (size_t)(head * 8 + w) * 4096;
  char* kvbuf = ws + WS_KV + (size_t)(head * 8 + w) * 16384;
  __builtin_amdgcn_s_setprio(1);
  unsigned ep;
  asm volatile("global_load_dword %0, %1, %2 sc1" : "=v"(ep) : "v"(0u), "s"(epw) : "memory");
  unsigned warm, warm2;
  asm volatile("s_load_dword %0, %2, 0x0\n\ts_load_dword %1, %2, 0x40" : "=&s"(warm), "=&s"(warm2) : "s"(pc0 + min((b >> 3) * 8 + w, (max(left_a, left_b) >> 7) - 1) * 128) : "memory");
  STAMP(1);

  const unsigned xr = r * 256 + ((h ^ (r & 15)) << 4);
  u32x4 om[2][8];
  wait_ge(lds + W_LANDED, P_K);
  asm volatile("" ::"s"(warm), "s"(warm2));
#pragma unroll
  for (int i = 0; i < 2; ++i)
#pragma unroll
    for (int t = 0; t < 8; ++t) om[i][t] = lds_rd128(((lds + LDS_OM + xr) ^ (t << 5)) + i * 8192);
  arrive(lds + W_OM);

  const float scale = 0.29730177875068026f;
  const float c1 = scale * 1.4426950408889634f;
  const float c2 = -0.5f * scale * scale * 1.4426950408889634f;

  {
    const unsigned slotA = lds + LDS_A + w * 8192;
    wait_ge(lds + W_LANDED, P_K + 8 * (w + 1));
    u32x4 kf[8];
    unsigned ka = slotA + xr;
    asm volatile("" : "+v"(ka));
#pragma unroll
    for (int t = 0; t < 8; ++t) kf[t] = lds_rd128(ka ^ (t << 5));
    SCHED_FENCE();
    f32x16 acc[2] = {};
    float ss[4] = {};
#pragma unroll
    for (int t = 0; t < 8; ++t)
#pragma unroll
      for (int i = 0; i < 4; ++i) ss[i] = dot2(kf[t][i], kf[t][i], ss[i]);
#pragma unroll
    for (int t = 0; t < 8; ++t) acc[0] = MFMA(as_frag(kf[t]), as_frag(om[0][t]), acc[0]);
    const float bias = add_halves((ss[0] + ss[1]) + (ss[2] + ss[3])) * c2 - 3.0f;
    *(LAS float*)(size_t)(slotA + 4096 + r * 4) = bias;
    f32x4 bz[4];
#pragma unroll
    for (int g = 0; g < 4; ++g) bz[g] = *(const LAS f32x4*)(size_t)(slotA + 4096 + (8 * g + 4 * h) * 4);
#pragma unroll
    for (int t = 0; t < 8; ++t) acc[1] = MFMA(as_frag(kf[t]), as_frag(om[1][t]), acc[1]);
#pragma unroll
    for (int i = 0; i < 2; ++i)
#pragma unroll
      for (int s = 0; s < 2; ++s) {
        u32x4 f;
#pragma unroll
        for (int j = 0; j < 4; ++j) {
          const int e = 8 * s + 2 * j;
          const float p0 = __builtin_amdgcn_exp2f(acc[i][e] * c1 + bz[e >> 2][e & 3]) + 1e-6f;
          const float p1 = __builtin_amdgcn_exp2f(acc[i][e + 1] * c1 + bz[(e + 1) >> 2][(e + 1) & 3]) + 1e-6f;
          f[j] = pk_bf16(p0, p1);
        }
        lds_wr128(slotA + (i * 2 + s) * 1024 + l16, f);
      }
  }
  arrive(lds + W_PHI);
  asm volatile("s_waitcnt vmcnt(0)" : "+v"(ep)::"memory");
  const unsigned tag = __builtin_amdgcn_readfirstlane(ep) + 1;
  const unsigned salt = tag + lane * 0x9E3779B1u;
  wait_ge(lds + W_PHI, 8);
  STAMP(4);

  f32x16 kv = {};
  float zown = 0.f;
  {
    const unsigned ab = lds + LDS_A + mt * 2048 + l16;
    u32x4 af[16];
#pragma unroll
    for (int ks = 0; ks < 10; ++ks) af[ks] = lds_rd128(ab + (ks >> 1) * 8192 + (ks & 1) * 1024);
    arrive(lds + W_AF);
    float zs[4] = {};
    const unsigned qq = (lane & 15) >> 2, p = lane & 3, blk = (lane >> 4) & 1;
    const unsigned trb = lds + LDS_B + (4 * h + qq) * 256 + (((4 * dt + 2 * blk + (p >> 1)) ^ (qq << 2)) << 4) + 8 * (p & 1);
#pragma unroll
    for (int c = 0; c < 4; ++c) {
      if (c == 1 || c == 2) {
#pragma unroll
        for (int ks = (c == 1 ? 10 : 12); ks < (c == 1 ? 12 : 16); ++ks) af[ks] = lds_rd128(ab + (ks >> 1) * 8192 + (ks & 1) * 1024);
      }
      if (zwave && c < 3) {
#pragma unroll
        for (int ks = (c == 0 ? 0 : 4 * c + 4); ks < 4 * c + 8; ++ks)
#pragma unroll
          for (int i = 0; i < 4; ++i) zs[i] = dot2(af[ks][i], 0x3F803F80u, zs[i]);
        if (c == 2) zown = add_halves((zs[0] + zs[1]) + (zs[2] + zs[3]));
      }
      SCHED_FENCE();
      wait_ge(lds + W_LANDED, P_V + 16 * (c + 1));
      if (c == 3) STAMP(12);
      s16x4 vf[8];
#pragma unroll
      for (int u = 0; u < 8; ++u) vf[u] = __builtin_amdgcn_ds_read_tr16_b64_v4i16((LAS s16x4*)(size_t)(trb + (64 * c + 8 * u) * 256));
      SCHED_FENCE();
#pragma unroll
      for (int u = 0; u < 4; ++u)
        kv = MFMA(as_frag(af[4 * c + u]), __builtin_bit_cast(bf16x8, __builtin_shufflevector(vf[2 * u], vf[2 * u + 1], 0, 1, 2, 3, 4, 5, 6, 7)), kv);
    }
    arrive(lds + W_KV);
  }
  {
    u32x4 d0, d1;
    pack_tile(kv, d0, d1);
    ST128_DEV(kvbuf + chunk * 2048, l16, d0, 0);
    ST128_DEV(kvbuf + chunk * 2048, l16, d1, 1024);
    const unsigned zb = __builtin_bit_cast(unsigned, zown);
    const u32x2 ax = {check_word(d0, d1, zb, salt), zb};
    if (zwave) asm volatile("global_store_dwordx2 %0, %1, %2 sc1" ::"v"(lane * 8), "v"(ax), "s"(auxbuf + chunk * 512) : "memory");
    else asm volatile("global_store_dword %0, %1, %2 sc1" ::"v"(lane * 4), "v"(ax[0]), "s"(auxbuf + chunk * 512) : "memory");
  }
  STAMP(5);

  if (w >= 4) __builtin_amdgcn_s_setprio(2);
  const unsigned slotQ = w < 3 ? lds + LDS_C + w * 8192 : lds + LDS_A + (w - 3) * 8192;
  u32x4 xq[4];
  {
    wait_ge(lds + W_LANDED, P_Q + 8 * (w + 1));
    STAMP(7);
    u32x4 qf[8];
    unsigned qa = slotQ + xr;
    asm volatile("" : "+v"(qa));
#pragma unroll
    for (int t = 0; t < 8; ++t) qf[t] = lds_rd128(qa ^ (t << 5));
    SCHED_FENCE();
    f32x16 acc[2] = {};
    float ss[4] = {};
#pragma unroll
    for (int t = 0; t < 8; ++t)
#pragma unroll
      for (int i = 0; i < 4; ++i) ss[i] = dot2(qf[t][i], qf[t][i], ss[i]);
#pragma unroll
    for (int t = 0; t < 8; ++t) acc[0] = MFMA(as_frag(om[0][t]), as_frag(qf[t]), acc[0]);
    const float bias = add_halves((ss[0] + ss[1]) + (ss[2] + ss[3])) * c2 - 3.0f;
#pragma unroll
    for (int t = 0; t < 8; ++t) acc[1] = MFMA(as_frag(om[1][t]), as_frag(qf[t]), acc[1]);
#pragma unroll
    for (int u = 0; u < 4; ++u)
#pragma unroll
      for (int j = 0; j < 4; ++j) {
        const int e = 8 * (u & 1) + 2 * j;
        xq[u][j] = pk_bf16(__builtin_amdgcn_exp2f(acc[u >> 1][e] * c1 + bias) + 1e-6f, __builtin_amdgcn_exp2f(acc[u >> 1][e + 1] * c1 + bias) + 1e-6f);
      }
    asm volatile("" : "+v"(xq[0]), "+v"(xq[1]), "+v"(xq[2]), "+v"(xq[3]));
    SCHED_FENCE();
  }
  unsigned wb = slotQ + r * 256 + 8 * (h ^ ((r >> 3) & 1)), wx = (r & 7) << 4;
  char* dst = out + (row0 + w * 32) * 256;
  unsigned so = (lane >> 3) * 256 + (lane & 7) * 16;
  unsigned rb = slotQ + (lane >> 3) * 256 + (((lane & 7) ^ (lane >> 3)) << 4);
  unsigned kb = lds + LDS_B + l16, zb = lds + LDS_Z + h * 16;
  asm volatile("" : "+v"(wb), "+v"(wx), "+v"(so), "+v"(rb), "+v"(kb), "+v"(zb), "+s"(dst));

  __builtin_amdgcn_s_setprio(1);
  wait_ge(lds + W_KV, 8);
  wait_ge(lds + W_LANDED, P_END - GATE);
  STAMP(6);
  {
    u32x4 pa[7], pb[7];
    u32x2 px[7];
    const unsigned xo = zwave ? lane * 8 : (lane >> 1) * 8;
    const bool hi = !zwave && (lane & 1);
#define REC_LOAD(i, TAIL, C)                                                                                          \
  asm volatile("global_load_dwordx4 %0, %3, %5 sc1\n\tglobal_load_dwordx4 %1, %3, %5 offset:1024 sc1\n\t"             \
               "global_load_dwordx2 %2, %4, %6 sc1" TAIL : C(pa[i]), C(pb[i]), C(px[i])                               \
               : "v"(l16), "v"(xo), "s"(kvbuf + ((chunk + 1 + (i)) & 7) * 2048), "s"(auxbuf + ((chunk + 1 + (i)) & 7) * 512) : "memory")
#define REC_BAD(i) (__builtin_amdgcn_ballot_w64(check_word(pa[i], pb[i], zwave ? px[i][1] : 0u, salt) != (hi ? px[i][1] : px[i][0])) != 0)
#pragma unroll
    for (int i = 0; i < 7; ++i) REC_LOAD(i, "", "=&v");
    if (w >= 4) __builtin_amdgcn_s_setprio(2);
    STAMP(14);
    const u32x4 id0 = lds_rd128(lds + LDS_ID + l16), id1 = lds_rd128(lds + LDS_ID + 1024 + l16);
    unsigned spin = 0;
#pragma unroll
    for (int i = 0; i < 7; ++i) {
      asm volatile("s_waitcnt vmcnt(%3)" : "+v"(pa[i]), "+v"(pb[i]), "+v"(px[i]) : "n"(18 - 3 * i) : "memory");
      if (__builtin_expect(REC_BAD(i), 0)) {
        do {
          COUNT_RETRY();
          __builtin_amdgcn_s_sleep(2);
          REC_LOAD(i, "\n\ts_waitcnt vmcnt(0)", "+v");
        } while (REC_BAD(i) && ++spin < SPIN_CAP);
      }
      kv = MFMA(as_frag(id0), as_frag(pa[i]), kv);
      kv = MFMA(as_frag(id1), as_frag(pb[i]), kv);
      const unsigned zu = px[i][1];
      zown += __builtin_bit_cast(float, zu);
    }
    STAMP(8);
    if (zwave && lane < 32)
      *(LAS unsigned short*)(size_t)(lds + LDS_Z + mt * 64 + (((r >> 4) * 2 + ((r >> 2) & 1)) * 8 + ((r >> 3) & 1) * 4 + (r & 3)) * 2) = (unsigned short)pk_bf16(zown, 0.f);
    u32x4 d0, d1;
    pack_tile(kv, d0, d1);
    lds_wr128(kb + (mt * 4 + dt) * 2048, d0);
    lds_wr128(kb + (mt * 4 + dt) * 2048 + 1024, d1);
    arrive(lds + W_H + (w >> 2) * 4);
  }
  STAMP(9);

  {
    float inv;
#pragma unroll
    for (int half = 0; half < 2; ++half) {
      wait_ge(lds + W_H + half * 4, 4);
      if (half) STAMP(3); else STAMP(10);
      u32x4 zf[4], kf2[2][4];
      if (half == 0) {
#pragma unroll
        for (int u = 0; u < 4; ++u) zf[u] = lds_rd128(zb + u * 32);
      }
#pragma unroll
      for (int d = 0; d < 2; ++d) {
        SCHED_FENCE();
#pragma unroll
        for (int u = 0; u < 4; ++u) kf2[d][u] = lds_rd128(kb + ((u >> 1) * 4 + 2 * half + d) * 2048 + (u & 1) * 1024);
      }
      SCHED_FENCE();
      f32x16 den = {}, o[2] = {};
      if (half == 0) {
#pragma unroll
        for (int u = 0; u < 4; ++u) den = MFMA(as_frag(zf[u]), as_frag(xq[u]), den);
      }
#pragma unroll
      for (int d = 0; d < 2; ++d)
#pragma unroll
        for (int u = 0; u < 4; ++u) o[d] = MFMA(as_frag(kf2[d][u]), as_frag(xq[u]), o[d]);
      if (half == 0) inv = __builtin_amdgcn_rcpf(den[0] + 1e-6f);
#pragma unroll
      for (int d = 0; d < 2; ++d)
#pragma unroll
        for (int g = 0; g < 4; ++g) {
          const u32x2 pk = {pk_bf16(o[d][4 * g] * inv, o[d][4 * g + 1] * inv), pk_bf16(o[d][4 * g + 2] * inv, o[d][4 * g + 3] * inv)};
          lds_wr64(wb + (((8 * half + 4 * d + g) << 4) ^ wx), pk);
        }
      u32x4 x[4];
#pragma unroll
      for (int i = 0; i < 4; ++i) x[i] = lds_rd128(rb + i * 2048 + half * 128);
#pragma unroll
      for (int i = 0; i < 4; ++i) {
        if (i & 1) x[i] = (u32x4){x[i][2], x[i][3], x[i][0], x[i][1]};
        ST128_DEV(dst, so + (i >> 1) * 4096, x[i], (i & 1) * 2048 + half * 128);
      }
      if (half == 0) STAMP(2);
    }
  }
  if (chunk == 0 && w == 0 && lane == 0)
    asm volatile("global_store_dword %0, %1, %2 sc1" ::"v"(0u), "v"(tag), "s"(epw) : "memory");
  STAMP(11);
  asm volatile(".Lrfa_end_b:");
}

extern "C" void kernel_launch(void* const* d_in, const int* in_sizes, int n_in, void* d_out, int out_size, void* d_ws,
                              size_t ws_size, hipStream_t stream) {
  rfa_kernel<<<256, 576, 0, stream>>>((const char*)d_in[0], (const char*)d_in[1], (const char*)d_in[2], (const char*)d_in[3],
                                      (char*)d_out, (char*)d_ws);
}
```

```cpp
#include <hip/hip_runtime.h>
#include <hip/hip_bf16.h>
#include <stdint.h>

typedef __bf16 bf16x8 __attribute__((ext_vector_type(8)));
typedef __bf16 bf16x2 __attribute__((ext_vector_type(2)));
typedef short s16x4 __attribute__((ext_vector_type(4)));
typedef float f32x16 __attribute__((ext_vector_type(16)));
typedef float f32x4 __attribute__((ext_vector_type(4)));
typedef unsigned u32x4 __attribute__((ext_vector_type(4)));
typedef unsigned u32x2 __attribute__((ext_vector_type(2)));
#define LAS __attribute__((address_space(3)))
#define DI __device__ __forceinline__

#define LD_POL "nt"
#define WIN 24
#define PRO 40
#define GATE 16
#define ROT 1

constexpr int S = 2048;
constexpr int LDS_A = 0;
constexpr int LDS_B = 65536;
constexpr int LDS_OM = LDS_B + 49152;
constexpr int LDS_C = 131072;
constexpr int LDS_D = 155648;
constexpr int W_LANDED = LDS_D, W_OM = LDS_D + 4, W_PHI = LDS_D + 8, W_AF = LDS_D + 12, W_KV = LDS_D + 16, W_H = LDS_D + 20;
constexpr int LDS_Z = LDS_D + 128, LDS_ID = LDS_D + 256;
constexpr int P_K = 16, P_V = 80, P_Q = 144, P_END = 208;
constexpr size_t WS_EP = 0;
constexpr size_t WS_AUX = 65536;
constexpr size_t WS_KV = WS_AUX + 1048576;
constexpr unsigned SPIN_CAP = 1u << 18;

#define STAMP(i)
#define COUNT_RETRY()
#define MFMA(a, b, c) __builtin_amdgcn_mfma_f32_32x32x16_bf16((a), (b), (c), 0, 0, 0)
#define WAIT_VM(n) asm volatile("s_waitcnt vmcnt(%0)" ::"n"(n) : "memory")
#define SCHED_FENCE() __builtin_amdgcn_sched_barrier(0)

DI unsigned pk_bf16(float lo, float hi) { bf16x2 p; p[0] = (__bf16)lo; p[1] = (__bf16)hi; return __builtin_bit_cast(unsigned, p); }
DI bf16x8 as_frag(u32x4 v) { return __builtin_bit_cast(bf16x8, v); }
DI u32x4 lds_rd128(unsigned a) { return *(const LAS u32x4*)(size_t)a; }
DI void lds_wr128(unsigned a, u32x4 v) { *(LAS u32x4*)(size_t)a = v; }
DI void lds_wr64(unsigned a, u32x2 v) { *(LAS u32x2*)(size_t)a = v; }
DI float dot2(unsigned a, unsigned b, float c) { return __builtin_amdgcn_fdot2_f32_bf16(__builtin_bit_cast(bf16x2, a), __builtin_bit_cast(bf16x2, b), c, false); }
DI float add_halves(float x) {
  const unsigned u = __builtin_bit_cast(unsigned, x);
  const auto r = __builtin_amdgcn_permlane32_swap(u, u, false, false);
  return __builtin_bit_cast(float, (unsigned)r[0]) + __builtin_bit_cast(float, (unsigned)r[1]);
}
DI void pack_tile(const f32x16& t, u32x4& d0, u32x4& d1) {
#pragma unroll
  for (int j = 0; j < 4; ++j) { d0[j] = pk_bf16(t[2 * j], t[2 * j + 1]); d1[j] = pk_bf16(t[8 + 2 * j], t[8 + 2 * j + 1]); }
}
DI unsigned rot(unsigned x, int k) { return ROT ? __builtin_amdgcn_alignbit(x, x, k) : x; }
DI unsigned check_word(u32x4 a, u32x4 b, unsigned z, unsigned salt) {
  return ((a[0] + rot(a[1], 3) + rot(a[2], 5)) + (rot(a[3], 7) + rot(b[0], 9) + rot(b[1], 11))) + ((rot(b[2], 13) + rot(b[3], 15) + z) + salt);
}

#define DMA_QUAD(lds, g, v0, v1, v2, v3, POL)                                                   \
  asm volatile("s_mov_b32 m0, %0\n\ts_nop 0\n\t"                                                \
               "global_load_lds_dwordx4 %2, %1 offset:0 " POL "\n\t"                            \
               "global_load_lds_dwordx4 %3, %1 offset:1024 " POL "\n\t"                         \
               "global_load_lds_dwordx4 %4, %1 offset:2048 " POL "\n\t"                         \
               "global_load_lds_dwordx4 %5, %1 offset:3072 " POL                                \
               ::"s"(lds), "s"(g), "v"(v0), "v"(v1), "v"(v2), "v"(v3) : "memory")
#define ST128_DEV(g, voff, val, off)                                                            \
  asm volatile("global_store_dwordx4 %0, %1, %2 offset:%3 sc1\n\ts_nop 1" ::"v"(voff), "v"(val), "s"(g), "n"(off) : "memory")

DI void wait_ge(unsigned addr, unsigned need) {
  for (;;) {
    unsigned x;
    asm volatile("ds_read_b32 %0, %1\n\ts_waitcnt lgkmcnt(0)" : "=v"(x) : "v"(addr) : "memory");
    if ((unsigned)__builtin_amdgcn_readfirstlane(x) >= need) break;
    __builtin_amdgcn_s_sleep(1);
  }
}
DI void arrive(unsigned addr) {
  unsigned long long ex;
  asm volatile("s_mov_b64 %0, exec\n\ts_mov_b64 exec, 1\n\tds_add_u32 %1, %2\n\ts_mov_b64 exec, %0" : "=&s"(ex) : "v"(addr), "v"(1u) : "memory");
}
DI void post(unsigned addr, unsigned val) { asm volatile("ds_write_b32 %0, %1" ::"v"(addr), "v"(val) : "memory"); }

__global__ __launch_bounds__(576, 3) void rfa_kernel(const char* __restrict__ q, const char* __restrict__ k,
                                                     const char* __restrict__ v, const char* __restrict__ omega,
                                                     char* __restrict__ out, char* ws) {
  __shared__ __attribute__((aligned(16))) char smem[163840];
  STAMP(0);
  unsigned long long pc0;
  int left_a, left_b;
  asm volatile("s_getpc_b64 %0\n.Lrfa_pc:\n\ts_mov_b32 %1, .Lrfa_end_a-.Lrfa_pc\n\ts_mov_b32 %2, .Lrfa_end_b-.Lrfa_pc" : "=s"(pc0), "=s"(left_a), "=s"(left_b));
  const unsigned lds = (unsigned)(size_t)(LAS char*)smem;
  const int w = __builtin_amdgcn_readfirstlane(threadIdx.x >> 6);
  const unsigned lane = threadIdx.x & 63, r = lane & 31, h = lane >> 5;
  const int b = blockIdx.x, head = (b & 7) * 4 + (b >> 6), chunk = (b >> 3) & 7;
  const size_t row0 = (size_t)head * S + chunk * 256;
  const unsigned l16 = lane * 16;
  unsigned vs[4];
#pragma unroll
  for (int i = 0; i < 4; ++i) vs[i] = (lane >> 4) * 256 + (((lane & 15) ^ ((4 * i + (lane >> 4)) & 15)) << 4);
  const char* kk = k + row0 * 256;
  if (w == 8) {
#pragma unroll 1
    for (int j = 0; j < 4; ++j) DMA_QUAD(lds + LDS_OM + j * 4096, omega + j * 4096, vs[0], vs[1], vs[2], vs[3], "");
#pragma unroll 1
    for (int j = 0; j < (PRO - P_K) / 4; ++j) DMA_QUAD(lds + LDS_A + j * 4096, kk + j * 4096, vs[0], vs[1], vs[2], vs[3], LD_POL);
    post(lds + LDS_D + (lane & 7) * 4, 0u);
  }
  if (w == 7) {
    const unsigned pos = ((r >> 2) & 1) == h ? ((r >> 4) << 2) | (((r >> 3) & 1) << 1) | ((r >> 1) & 1) : 8u;
    const unsigned one = (r & 1) ? 0x3F800000u : 0x3F80u;
    u32x4 id0, id1;
#pragma unroll
    for (int j = 0; j < 4; ++j) { id0[j] = pos == j ? one : 0u; id1[j] = pos == 4 + j ? one : 0u; }
    lds_wr128(lds + LDS_ID + l16, id0);
    lds_wr128(lds + LDS_ID + 1024 + l16, id1);
  }
  asm volatile("s_waitcnt lgkmcnt(0)\n\ts_barrier" ::: "memory");

  if (w == 8) {
    const unsigned vv = (lane >> 4) * 256 + (((lane & 15) ^ ((lane >> 4) << 2)) << 4);
    const char* vsrc = v + row0 * 256;
    const char* qq = q + row0 * 256;
#pragma unroll
    for (int n = PRO - 16; n > WIN - 4; n -= 4) {
      WAIT_VM(n);
      post(lds + W_LANDED, PRO - n);
    }
    unsigned done = PRO - WIN;
    auto run = [&](int quads, unsigned dst, const char* src, unsigned v0, unsigned v1, unsigned v2, unsigned v3) {
#pragma unroll 1
      for (int j = 0; j < quads; ++j) {
        DMA_QUAD(dst, src, v0, v1, v2, v3, LD_POL);
        dst += 4096; src += 4096; done += 4;
        WAIT_VM(WIN);
        post(lds + W_LANDED, done);
      }
    };
    run((P_V - PRO) / 4, lds + LDS_A + (PRO - P_K) * 1024, kk + (PRO - P_K) * 1024, vs[0], vs[1], vs[2], vs[3]);
    STAMP(1);
    run(12, lds + LDS_B, vsrc, vv, vv, vv, vv);
    wait_ge(lds + W_OM, 8);
    run(4, lds + LDS_B + 49152, vsrc + 49152, vv, vv, vv, vv);
    STAMP(2);
    run(6, lds + LDS_C, qq, vs[0], vs[1], vs[2], vs[3]);
    wait_ge(lds + W_AF, 8);
    run(10, lds + LDS_A, qq + 24576, vs[0], vs[1], vs[2], vs[3]);
    STAMP(3);
#pragma unroll
    for (int n = WIN - 4; n >= 0; n -= 4) {
      WAIT_VM(n);
      post(lds + W_LANDED, P_END - n);
    }
    STAMP(4);
    asm volatile(".Lrfa_end_a:");
    return;
  }

  const int mt = (w >> 1) & 1, dt = (w & 1) | ((w >> 2) << 1);
  const bool zwave = dt == 0;
  char* epw = ws + WS_EP + head * 128;
  char* auxbuf = ws + WS_AUX + (size_t)(head * 8 + w) * 4096;
  char* kvbuf = ws + WS_KV + (size_t)(head * 8 + w) * 16384;
  __builtin_amdgcn_s_setprio(1);
  unsigned ep;
  asm volatile("global_load_dword %0, %1, %2 sc1" : "=v"(ep) : "v"(0u), "s"(epw) : "memory");
  unsigned warm, warm2;
  asm volatile("s_load_dword %0, %2, 0x0\n\ts_load_dword %1, %2, 0x40" : "=&s"(warm), "=&s"(warm2) : "s"(pc0 + min((b >> 3) * 8 + w, (max(left_a, left_b) >> 7) - 1) * 128) : "memory");
  STAMP(1);

  const unsigned xr = r * 256 + ((h ^ (r & 15)) << 4);
  u32x4 om[2][8];
  wait_ge(lds + W_LANDED, P_K);
  asm volatile("" ::"s"(warm), "s"(warm2));
#pragma unroll
  for (int i = 0; i < 2; ++i)
#pragma unroll
    for (int t = 0; t < 8; ++t) om[i][t] = lds_rd128(((lds + LDS_OM + xr) ^ (t << 5)) + i * 8192);
  arrive(lds + W_OM);

  const float scale = 0.29730177875068026f;
  const float c1 = scale * 1.4426950408889634f;
  const float c2 = -0.5f * scale * scale * 1.4426950408889634f;

  {
    const unsigned slotA = lds + LDS_A + w * 8192;
    wait_ge(lds + W_LANDED, P_K + 8 * (w + 1));
    u32x4 kf[8];
    unsigned ka = slotA + xr;
    asm volatile("" : "+v"(ka));
#pragma unroll
    for (int t = 0; t < 8; ++t) kf[t] = lds_rd128(ka ^ (t << 5));
    SCHED_FENCE();
    f32x16 acc[2] = {};
    float ss[4] = {};
#pragma unroll
    for (int t = 0; t < 8; ++t)
#pragma unroll
      for (int i = 0; i < 4; ++i) ss[i] = dot2(kf[t][i], kf[t][i], ss[i]);
#pragma unroll
    for (int t = 0; t < 8; ++t) acc[0] = MFMA(as_frag(kf[t]), as_frag(om[0][t]), acc[0]);
    const float bias = add_halves((ss[0] + ss[1]) + (ss[2] + ss[3])) * c2 - 3.0f;
    *(LAS float*)(size_t)(slotA + 4096 + r * 4) = bias;
    f32x4 bz[4];
#pragma unroll
    for (int g = 0; g < 4; ++g) bz[g] = *(const LAS f32x4*)(size_t)(slotA + 4096 + (8 * g + 4 * h) * 4);
#pragma unroll
    for (int t = 0; t < 8; ++t) acc[1] = MFMA(as_frag(kf[t]), as_frag(om[1][t]), acc[1]);
#pragma unroll
    for (int i = 0; i < 2; ++i)
#pragma unroll
      for (int s = 0; s < 2; ++s) {
        u32x4 f;
#pragma unroll
        for (int j = 0; j < 4; ++j) {
          const int e = 8 * s + 2 * j;
          const float p0 = __builtin_amdgcn_exp2f(acc[i][e] * c1 + bz[e >> 2][e & 3]) + 1e-6f;
          const float p1 = __builtin_amdgcn_exp2f(acc[i][e + 1] * c1 + bz[(e + 1) >> 2][(e + 1) & 3]) + 1e-6f;
          f[j] = pk_bf16(p0, p1);
        }
        lds_wr128(slotA + (i * 2 + s) * 1024 + l16, f);
      }
  }
  arrive(lds + W_PHI);
  asm volatile("s_waitcnt vmcnt(0)" : "+v"(ep)::"memory");
  const unsigned tag = __builtin_amdgcn_readfirstlane(ep) + 1;
  const unsigned salt = tag + lane * 0x9E3779B1u;
  wait_ge(lds + W_PHI, 8);
  STAMP(4);

  f32x16 kv = {};
  float zown = 0.f;
  {
    const unsigned ab = lds + LDS_A + mt * 2048 + l16;
    u32x4 af[16];
#pragma unroll
    for (int ks = 0; ks < 10; ++ks) af[ks] = lds_rd128(ab + (ks >> 1) * 8192 + (ks & 1) * 1024);
    arrive(lds + W_AF);
    float zs[4] = {};
    const unsigned qq = (lane & 15) >> 2, p = lane & 3, blk = (lane >> 4) & 1;
    const unsigned trb = lds + LDS_B + (4 * h + qq) * 256 + (((4 * dt + 2 * blk + (p >> 1)) ^ (qq << 2)) << 4) + 8 * (p & 1);
#pragma unroll
    for (int c = 0; c < 4; ++c) {
      if (c == 1 || c == 2) {
#pragma unroll
        for (int ks = (c == 1 ? 10 : 12); ks < (c == 1 ? 12 : 16); ++ks) af[ks] = lds_rd128(ab + (ks >> 1) * 8192 + (ks & 1) * 1024);
      }
      if (zwave && c < 3) {
#pragma unroll
        for (int ks = (c == 0 ? 0 : 4 * c + 4); ks < 4 * c + 8; ++ks)
#pragma unroll
          for (int i = 0; i < 4; ++i) zs[i] = dot2(af[ks][i], 0x3F803F80u, zs[i]);
        if (c == 2) zown = add_halves((zs[0] + zs[1]) + (zs[2] + zs[3]));
      }
      SCHED_FENCE();
      wait_ge(lds + W_LANDED, P_V + 16 * (c + 1));
      if (c == 3) STAMP(12);
      s16x4 vf[8];
#pragma unroll
      for (int u = 0; u < 8; ++u) vf[u] = __builtin_amdgcn_ds_read_tr16_b64_v4i16((LAS s16x4*)(size_t)(trb + (64 * c + 8 * u) * 256));
      SCHED_FENCE();
#pragma unroll
      for (int u = 0; u < 4; ++u)
        kv = MFMA(as_frag(af[4 * c + u]), __builtin_bit_cast(bf16x8, __builtin_shufflevector(vf[2 * u], vf[2 * u + 1], 0, 1, 2, 3, 4, 5, 6, 7)), kv);
    }
    arrive(lds + W_KV);
  }
  {
    u32x4 d0, d1;
    pack_tile(kv, d0, d1);
    ST128_DEV(kvbuf + chunk * 2048, l16, d0, 0);
    ST128_DEV(kvbuf + chunk * 2048, l16, d1, 1024);
    const unsigned zb = __builtin_bit_cast(unsigned, zown);
    const u32x2 ax = {check_word(d0, d1, zb, salt), zb};
    if (zwave) asm volatile("global_store_dwordx2 %0, %1, %2 sc1" ::"v"(lane * 8), "v"(ax), "s"(auxbuf + chunk * 512) : "memory");
    else asm volatile("global_store_dword %0, %1, %2 sc1" ::"v"(lane * 4), "v"(ax[0]), "s"(auxbuf + chunk * 512) : "memory");
  }
  STAMP(5);

  if (w >= 4) __builtin_amdgcn_s_setprio(2);
  const unsigned slotQ = w < 3 ? lds + LDS_C + w * 8192 : lds + LDS_A + (w - 3) * 8192;
  u32x4 xq[4];
  {
    wait_ge(lds + W_LANDED, P_Q + 8 * (w + 1));
    STAMP(7);
    u32x4 qf[8];
    unsigned qa = slotQ + xr;
    asm volatile("" : "+v"(qa));
#pragma unroll
    for (int t = 0; t < 8; ++t) qf[t] = lds_rd128(qa ^ (t << 5));
    SCHED_FENCE();
    f32x16 acc[2] = {};
    float ss[4] = {};
#pragma unroll
    for (int t = 0; t < 8; ++t)
#pragma unroll
      for (int i = 0; i < 4; ++i) ss[i] = dot2(qf[t][i], qf[t][i], ss[i]);
#pragma unroll
    for (int t = 0; t < 8; ++t) acc[0] = MFMA(as_frag(om[0][t]), as_frag(qf[t]), acc[0]);
    const float bias = add_halves((ss[0] + ss[1]) + (ss[2] + ss[3])) * c2 - 3.0f;
#pragma unroll
    for (int t = 0; t < 8; ++t) acc[1] = MFMA(as_frag(om[1][t]), as_frag(qf[t]), acc[1]);
#pragma unroll
    for (int u = 0; u < 4; ++u)
#pragma unroll
      for (int j = 0; j < 4; ++j) {
        const int e = 8 * (u & 1) + 2 * j;
        xq[u][j] = pk_bf16(__builtin_amdgcn_exp2f(acc[u >> 1][e] * c1 + bias) + 1e-6f, __builtin_amdgcn_exp2f(acc[u >> 1][e + 1] * c1 + bias) + 1e-6f);
      }
    asm volatile("" : "+v"(xq[0]), "+v"(xq[1]), "+v"(xq[2]), "+v"(xq[3]));
    SCHED_FENCE();
  }
  unsigned wb = slotQ + r * 256 + 8 * (h ^ ((r >> 3) & 1)), wx = (r & 7) << 4;
  char* dst = out + (row0 + w * 32) * 256;
  unsigned so = (lane >> 3) * 256 + (lane & 7) * 16;
  unsigned rb = slotQ + (lane >> 3) * 256 + (((lane & 7) ^ (lane >> 3)) << 4);
  unsigned kb = lds + LDS_B + l16, zb = lds + LDS_Z + h * 16;
  asm volatile("" : "+v"(wb), "+v"(wx), "+v"(so), "+v"(rb), "+v"(kb), "+v"(zb), "+s"(dst));

  __builtin_amdgcn_s_setprio(1);
  wait_ge(lds + W_KV, 8);
  wait_ge(lds + W_LANDED, P_END - GATE);
  STAMP(6);
  {
    u32x4 pa[7], pb[7];
    u32x2 px[7];
    const unsigned xo = zwave ? lane * 8 : (lane >> 1) * 8;
    const bool hi = !zwave && (lane & 1);
#define REC_LOAD(i, TAIL, C)                                                                                          \
  asm volatile("global_load_dwordx4 %0, %3, %5 sc1\n\tglobal_load_dwordx4 %1, %3, %5 offset:1024 sc1\n\t"             \
               "global_load_dwordx2 %2, %4, %6 sc1" TAIL : C(pa[i]), C(pb[i]), C(px[i])                               \
               : "v"(l16), "v"(xo), "s"(kvbuf + ((chunk + 1 + (i)) & 7) * 2048), "s"(auxbuf + ((chunk + 1 + (i)) & 7) * 512) : "memory")
#define REC_BAD(i) (__builtin_amdgcn_ballot_w64(check_word(pa[i], pb[i], zwave ? px[i][1] : 0u, salt) != (hi ? px[i][1] : px[i][0])) != 0)
#pragma unroll
    for (int i = 0; i < 7; ++i) REC_LOAD(i, "", "=&v");
    if (w >= 4) __builtin_amdgcn_s_setprio(2);
    STAMP(14);
    const u32x4 id0 = lds_rd128(lds + LDS_ID + l16), id1 = lds_rd128(lds + LDS_ID + 1024 + l16);
    unsigned spin = 0;
#pragma unroll
    for (int i = 0; i < 7; ++i) {
      asm volatile("s_waitcnt vmcnt(%3)" : "+v"(pa[i]), "+v"(pb[i]), "+v"(px[i]) : "n"(18 - 3 * i) : "memory");
      if (__builtin_expect(REC_BAD(i), 0)) {
        do {
          COUNT_RETRY();
          __builtin_amdgcn_s_sleep(2);
          REC_LOAD(i, "\n\ts_waitcnt vmcnt(0)", "+v");
        } while (REC_BAD(i) && ++spin < SPIN_CAP);
      }
      kv = MFMA(as_frag(id0), as_frag(pa[i]), kv);
      kv = MFMA(as_frag(id1), as_frag(pb[i]), kv);
      const unsigned zu = px[i][1];
      zown += __builtin_bit_cast(float, zu);
    }
    STAMP(8);
    if (zwave && lane < 32)
      *(LAS unsigned short*)(size_t)(lds + LDS_Z + mt * 64 + (((r >> 4) * 2 + ((r >> 2) & 1)) * 8 + ((r >> 3) & 1) * 4 + (r & 3)) * 2) = (unsigned short)pk_bf16(zown, 0.f);
    u32x4 d0, d1;
    pack_tile(kv, d0, d1);
    lds_wr128(kb + (mt * 4 + dt) * 2048, d0);
    lds_wr128(kb + (mt * 4 + dt) * 2048 + 1024, d1);
    arrive(lds + W_H + (w >> 2) * 4);
  }
  STAMP(9);

  {
    float inv;
#pragma unroll
    for (int half = 0; half < 2; ++half) {
      wait_ge(lds + W_H + half * 4, 4);
      if (half) STAMP(3); else STAMP(10);
      u32x4 zf[4], kf2[2][4];
      if (half == 0) {
#pragma unroll
        for (int u = 0; u < 4; ++u) zf[u] = lds_rd128(zb + u * 32);
      }
#pragma unroll
      for (int d = 0; d < 2; ++d) {
        SCHED_FENCE();
#pragma unroll
        for (int u = 0; u < 4; ++u) kf2[d][u] = lds_rd128(kb + ((u >> 1) * 4 + 2 * half + d) * 2048 + (u & 1) * 1024);
      }
      SCHED_FENCE();
      f32x16 den = {}, o[2] = {};
      if (half == 0) {
#pragma unroll
        for (int u = 0; u < 4; ++u) den = MFMA(as_frag(zf[u]), as_frag(xq[u]), den);
      }
#pragma unroll
      for (int d = 0; d < 2; ++d)
#pragma unroll
        for (int u = 0; u < 4; ++u) o[d] = MFMA(as_frag(kf2[d][u]), as_frag(xq[u]), o[d]);
      if (half == 0) inv = __builtin_amdgcn_rcpf(den[0] + 1e-6f);
#pragma unroll
      for (int d = 0; d < 2; ++d)
#pragma unroll
        for (int g = 0; g < 4; ++g) {
          const u32x2 pk = {pk_bf16(o[d][4 * g] * inv, o[d][4 * g + 1] * inv), pk_bf16(o[d][4 * g + 2] * inv, o[d][4 * g + 3] * inv)};
          lds_wr64(wb + (((8 * half + 4 * d + g) << 4) ^ wx), pk);
        }
      u32x4 x[4];
#pragma unroll
      for (int i = 0; i < 4; ++i) x[i] = lds_rd128(rb + i * 2048 + half * 128);
#pragma unroll
      for (int i = 0; i < 4; ++i) {
        if (i & 1) x[i] = (u32x4){x[i][2], x[i][3], x[i][0], x[i][1]};
        ST128_DEV(dst, so + (i >> 1) * 4096, x[i], (i & 1) * 2048 + half * 128);
      }
      if (half == 0) STAMP(2);
    }
  }
  if (chunk == 0 && w == 0 && lane == 0)
    asm volatile("global_store_dword %0, %1, %2 sc1" ::"v"(0u), "v"(tag), "s"(epw) : "memory");
  STAMP(11);
  asm volatile(".Lrfa_end_b:");
}

extern "C" void kernel_launch(void* const* d_in, const int* in_sizes, int n_in, void* d_out, int out_size, void* d_ws,
                              size_t ws_size, hipStream_t stream) {
  rfa_kernel<<<256, 576, 0, stream>>>((const char*)d_in[0], (const char*)d_in[1], (const char*)d_in[2], (const char*)d_in[3],
                                      (char*)d_out, (char*)d_ws);
}
```

```cpp
#include <hip/hip_runtime.h>
#include <hip/hip_bf16.h>
#include <stdint.h>

typedef __bf16 bf16x8 __attribute__((ext_vector_type(8)));
typedef __bf16 bf16x2 __attribute__((ext_vector_type(2)));
typedef short s16x4 __attribute__((ext_vector_type(4)));
typedef float f32x16 __attribute__((ext_vector_type(16)));
typedef float f32x4 __attribute__((ext_vector_type(4)));
typedef unsigned u32x4 __attribute__((ext_vector_type(4)));
typedef unsigned u32x2 __attribute__((ext_vector_type(2)));
#define LAS __attribute__((address_space(3)))
#define DI __device__ __forceinline__

#define LD_POL "nt"
constexpr int WIN = 24;
constexpr int PRO = 40;
constexpr int GATE = 16;

constexpr int S = 2048;
constexpr int LDS_A = 0;
constexpr int LDS_B = 65536;
constexpr int LDS_OM = LDS_B + 49152;
constexpr int LDS_C = 131072;
constexpr int LDS_D = 155648;
constexpr int W_LANDED = LDS_D, W_OM = LDS_D + 4, W_PHI = LDS_D + 8, W_AF = LDS_D + 12, W_KV = LDS_D + 16, W_H = LDS_D + 20;
constexpr int LDS_Z = LDS_D + 128, LDS_ID = LDS_D + 256;
constexpr int P_K = 16, P_V = 80, P_Q = 144, P_END = 208;
constexpr size_t WS_EP = 0;
constexpr size_t WS_AUX = 65536;
constexpr size_t WS_KV = WS_AUX + 1048576;
constexpr unsigned SPIN_CAP = 1u << 18;

#define MFMA(a, b, c) __builtin_amdgcn_mfma_f32_32x32x16_bf16((a), (b), (c), 0, 0, 0)
#define WAIT_VM(n) asm volatile("s_waitcnt vmcnt(%0)" ::"n"(n) : "memory")
#define SCHED_FENCE() __builtin_amdgcn_sched_barrier(0)

DI unsigned pk_bf16(float lo, float hi) { bf16x2 p; p[0] = (__bf16)lo; p[1] = (__bf16)hi; return __builtin_bit_cast(unsigned, p); }
DI bf16x8 as_frag(u32x4 v) { return __builtin_bit_cast(bf16x8, v); }
DI u32x4 lds_rd128(unsigned a) { return *(const LAS u32x4*)(size_t)a; }
DI void lds_wr128(unsigned a, u32x4 v) { *(LAS u32x4*)(size_t)a = v; }
DI void lds_wr64(unsigned a, u32x2 v) { *(LAS u32x2*)(size_t)a = v; }
DI float dot2(unsigned a, unsigned b, float c) { return __builtin_amdgcn_fdot2_f32_bf16(__builtin_bit_cast(bf16x2, a), __builtin_bit_cast(bf16x2, b), c, false); }
DI float add_halves(float x) {
  const unsigned u = __builtin_bit_cast(unsigned, x);
  const auto r = __builtin_amdgcn_permlane32_swap(u, u, false, false);
  return __builtin_bit_cast(float, (unsigned)r[0]) + __builtin_bit_cast(float, (unsigned)r[1]);
}
DI void pack_tile(const f32x16& t, u32x4& d0, u32x4& d1) {
#pragma unroll
  for (int j = 0; j < 4; ++j) { d0[j] = pk_bf16(t[2 * j], t[2 * j + 1]); d1[j] = pk_bf16(t[8 + 2 * j], t[8 + 2 * j + 1]); }
}
DI unsigned rot(unsigned x, int k) { return __builtin_amdgcn_alignbit(x, x, k); }
DI unsigned check_word(u32x4 a, u32x4 b, unsigned z, unsigned salt) {
  return ((a[0] + rot(a[1], 3) + rot(a[2], 5)) + (rot(a[3], 7) + rot(b[0], 9) + rot(b[1], 11))) + ((rot(b[2], 13) + rot(b[3], 15) + z) + salt);
}

#define DMA_QUAD(lds, g, v0, v1, v2, v3, POL)                                                   \
  asm volatile("s_mov_b32 m0, %0\n\ts_nop 0\n\t"                                                \
               "global_load_lds_dwordx4 %2, %1 offset:0 " POL "\n\t"                            \
               "global_load_lds_dwordx4 %3, %1 offset:1024 " POL "\n\t"                         \
               "global_load_lds_dwordx4 %4, %1 offset:2048 " POL "\n\t"                         \
               "global_load_lds_dwordx4 %5, %1 offset:3072 " POL                                \
               ::"s"(lds), "s"(g), "v"(v0), "v"(v1), "v"(v2), "v"(v3) : "memory")
#define ST128_DEV(g, voff, val, off)                                                            \
  asm volatile("global_store_dwordx4 %0, %1, %2 offset:%3 sc1\n\ts_nop 1" ::"v"(voff), "v"(val), "s"(g), "n"(off) : "memory")

DI void wait_ge(unsigned addr, unsigned need) {
  for (;;) {
    unsigned x;
    asm volatile("ds_read_b32 %0, %1\n\ts_waitcnt lgkmcnt(0)" : "=v"(x) : "v"(addr) : "memory");
    if ((unsigned)__builtin_amdgcn_readfirstlane(x) >= need) break;
    __builtin_amdgcn_s_sleep(1);
  }
}
DI void arrive(unsigned addr) {
  unsigned long long ex;
  asm volatile("s_mov_b64 %0, exec\n\ts_mov_b64 exec, 1\n\tds_add_u32 %1, %2\n\ts_mov_b64 exec, %0" : "=&s"(ex) : "v"(addr), "v"(1u) : "memory");
}
DI void post(unsigned addr, unsigned val) { asm volatile("ds_write_b32 %0, %1" ::"v"(addr), "v"(val) : "memory"); }

__global__ __launch_bounds__(576, 3) void rfa_kernel(const char* __restrict__ q, const char* __restrict__ k,
                                                     const char* __restrict__ v, const char* __restrict__ omega,
                                                     char* __restrict__ out, char* ws) {
  __shared__ __attribute__((aligned(16))) char smem[163840];
  const unsigned lds = (unsigned)(size_t)(LAS char*)smem;
  const int w = __builtin_amdgcn_readfirstlane(threadIdx.x >> 6);
  const unsigned lane = threadIdx.x & 63;
  const unsigned vs0 = (lane >> 4) * 256 + (((lane & 15) ^ (lane >> 4)) << 4), vs1 = vs0 ^ 64, vs2 = vs0 ^ 128, vs3 = vs0 ^ 192;
  if (w == 8) {
#pragma unroll 1
    for (int j = 0; j < 4; ++j) DMA_QUAD(lds + LDS_OM + j * 4096, omega + j * 4096, vs0, vs1, vs2, vs3, "");
  }
  const int b = blockIdx.x, head = (b & 7) * 4 + (b >> 6), chunk = (b >> 3) & 7;
  const size_t row0 = (size_t)head * S + chunk * 256;
  const char* kk = k + row0 * 256;
  if (w == 8) {
#pragma unroll 1
    for (int j = 0; j < (PRO - P_K) / 4; ++j) DMA_QUAD(lds + LDS_A + j * 4096, kk + j * 4096, vs0, vs1, vs2, vs3, LD_POL);
    post(lds + LDS_D + (lane & 7) * 4, 0u);
  }
  const unsigned r = lane & 31, h = lane >> 5, l16 = lane * 16;
  unsigned long long pc0;
  int left_a, left_b;
  asm volatile("s_getpc_b64 %0\n.Lrfa_pc:\n\ts_mov_b32 %1, .Lrfa_end_a-.Lrfa_pc\n\ts_mov_b32 %2, .Lrfa_end_b-.Lrfa_pc" : "=s"(pc0), "=s"(left_a), "=s"(left_b));
  if (w == 7) {
    const unsigned pos = ((r >> 2) & 1) == h ? ((r >> 4) << 2) | (((r >> 3) & 1) << 1) | ((r >> 1) & 1) : 8u;
    const unsigned one = (r & 1) ? 0x3F800000u : 0x3F80u;
    u32x4 id0, id1;
#pragma unroll
    for (int j = 0; j < 4; ++j) { id0[j] = pos == j ? one : 0u; id1[j] = pos == 4 + j ? one : 0u; }
    lds_wr128(lds + LDS_ID + l16, id0);
    lds_wr128(lds + LDS_ID + 1024 + l16, id1);
  }
  asm volatile("s_waitcnt lgkmcnt(0)\n\ts_barrier" ::: "memory");

  if (w == 8) {
    const unsigned vv = (lane >> 4) * 256 + (((lane & 15) ^ ((lane >> 4) << 2)) << 4);
    const char* vsrc = v + row0 * 256;
    const char* qq = q + row0 * 256;
#pragma unroll
    for (int n = PRO - 16; n > WIN - 4; n -= 4) {
      WAIT_VM(n);
      post(lds + W_LANDED, PRO - n);
    }
    unsigned done = PRO - WIN;
    auto run = [&](int quads, unsigned dst, const char* src, unsigned v0, unsigned v1, unsigned v2, unsigned v3) {
#pragma unroll 1
      for (int j = 0; j < quads; ++j) {
        DMA_QUAD(dst, src, v0, v1, v2, v3, LD_POL);
        dst += 4096; src += 4096; done += 4;
        WAIT_VM(WIN);
        post(lds + W_LANDED, done);
      }
    };
    run((P_V - PRO) / 4, lds + LDS_A + (PRO - P_K) * 1024, kk + (PRO - P_K) * 1024, vs0, vs1, vs2, vs3);
    run(12, lds + LDS_B, vsrc, vv, vv, vv, vv);
    wait_ge(lds + W_OM, 8);
    run(4, lds + LDS_B + 49152, vsrc + 49152, vv, vv, vv, vv);
    run(6, lds + LDS_C, qq, vs0, vs1, vs2, vs3);
    wait_ge(lds + W_AF, 8);
    run(10, lds + LDS_A, qq + 24576, vs0, vs1, vs2, vs3);
#pragma unroll
    for (int n = WIN - 4; n >= 0; n -= 4) {
      WAIT_VM(n);
      post(lds + W_LANDED, P_END - n);
    }
    asm volatile(".Lrfa_end_a:");
    return;
  }

  const int mt = (w >> 1) & 1, dt = (w & 1) | ((w >> 2) << 1);
  const bool zwave = dt == 0;
  char* epw = ws + WS_EP + head * 128;
  char* auxbuf = ws + WS_AUX + (size_t)(head * 8 + w) * 4096;
  char* kvbuf = ws + WS_KV + (size_t)(head * 8 + w) * 16384;
  __builtin_amdgcn_s_setprio(1);
  unsigned ep;
  asm volatile("global_load_dword %0, %1, %2 sc1" : "=v"(ep) : "v"(0u), "s"(epw) : "memory");
  unsigned warm, warm2;
  asm volatile("s_load_dword %0, %2, 0x0\n\ts_load_dword %1, %2, 0x40" : "=&s"(warm), "=&s"(warm2) : "s"(pc0 + min((b >> 3) * 8 + w, (max(left_a, left_b) >> 7) - 1) * 128) : "memory");

  const unsigned xr = r * 256 + ((h ^ (r & 15)) << 4);
  u32x4 om[2][8];
  wait_ge(lds + W_LANDED, P_K);
  asm volatile("" ::"s"(warm), "s"(warm2));
#pragma unroll
  for (int i = 0; i < 2; ++i)
#pragma unroll
    for (int t = 0; t < 8; ++t) om[i][t] = lds_rd128(((lds + LDS_OM + xr) ^ (t << 5)) + i * 8192);
  arrive(lds + W_OM);

  const float scale = 0.29730177875068026f;
  const float c1 = scale * 1.4426950408889634f;
  const float c2 = -0.5f * scale * scale * 1.4426950408889634f;

  {
    const unsigned slotA = lds + LDS_A + w * 8192;
    wait_ge(lds + W_LANDED, P_K + 8 * (w + 1));
    u32x4 kf[8];
    unsigned ka = slotA + xr;
    asm volatile("" : "+v"(ka));
#pragma unroll
    for (int t = 0; t < 8; ++t) kf[t] = lds_rd128(ka ^ (t << 5));
    SCHED_FENCE();
    f32x16 acc[2] = {};
    float ss[4] = {};
#pragma unroll
    for (int t = 0; t < 8; ++t)
#pragma unroll
      for (int i = 0; i < 4; ++i) ss[i] = dot2(kf[t][i], kf[t][i], ss[i]);
#pragma unroll
    for (int t = 0; t < 8; ++t) acc[0] = MFMA(as_frag(kf[t]), as_frag(om[0][t]), acc[0]);
    const float bias = add_halves((ss[0] + ss[1]) + (ss[2] + ss[3])) * c2 - 3.0f;
    *(LAS float*)(size_t)(slotA + 4096 + r * 4) = bias;
    f32x4 bz[4];
#pragma unroll
    for (int g = 0; g < 4; ++g) bz[g] = *(const LAS f32x4*)(size_t)(slotA + 4096 + (8 * g + 4 * h) * 4);
#pragma unroll
    for (int t = 0; t < 8; ++t) acc[1] = MFMA(as_frag(kf[t]), as_frag(om[1][t]), acc[1]);
#pragma unroll
    for (int i = 0; i < 2; ++i)
#pragma unroll
      for (int s = 0; s < 2; ++s) {
        u32x4 f;
#pragma unroll
        for (int j = 0; j < 4; ++j) {
          const int e = 8 * s + 2 * j;
          const float p0 = __builtin_amdgcn_exp2f(acc[i][e] * c1 + bz[e >> 2][e & 3]) + 1e-6f;
          const float p1 = __builtin_amdgcn_exp2f(acc[i][e + 1] * c1 + bz[(e + 1) >> 2][(e + 1) & 3]) + 1e-6f;
          f[j] = pk_bf16(p0, p1);
        }
        lds_wr128(slotA + (i * 2 + s) * 1024 + l16, f);
      }
  }
  arrive(lds + W_PHI);
  asm volatile("s_waitcnt vmcnt(0)" : "+v"(ep)::"memory");
  const unsigned tag = __builtin_amdgcn_readfirstlane(ep) + 1;
  const unsigned salt = tag + lane * 0x9E3779B1u;
  wait_ge(lds + W_PHI, 8);

  f32x16 kv = {};
  float zown = 0.f;
  {
    const unsigned ab = lds + LDS_A + mt * 2048 + l16;
    u32x4 af[16];
#pragma unroll
    for (int ks = 0; ks < 10; ++ks) af[ks] = lds_rd128(ab + (ks >> 1) * 8192 + (ks & 1) * 1024);
    arrive(lds + W_AF);
    float zs[4] = {};
    const unsigned qq = (lane & 15) >> 2, p = lane & 3, blk = (lane >> 4) & 1;
    const unsigned trb = lds + LDS_B + (4 * h + qq) * 256 + (((4 * dt + 2 * blk + (p >> 1)) ^ (qq << 2)) << 4) + 8 * (p & 1);
#pragma unroll
    for (int c = 0; c < 4; ++c) {
      if (c == 1 || c == 2) {
#pragma unroll
        for (int ks = (c == 1 ? 10 : 12); ks < (c == 1 ? 12 : 16); ++ks) af[ks] = lds_rd128(ab + (ks >> 1) * 8192 + (ks & 1) * 1024);
      }
      if (zwave && c < 3) {
#pragma unroll
        for (int ks = (c == 0 ? 0 : 4 * c + 4); ks < 4 * c + 8; ++ks)
#pragma unroll
          for (int i = 0; i < 4; ++i) zs[i] = dot2(af[ks][i], 0x3F803F80u, zs[i]);
        if (c == 2) zown = add_halves((zs[0] + zs[1]) + (zs[2] + zs[3]));
      }
      SCHED_FENCE();
      wait_ge(lds + W_LANDED, P_V + 16 * (c + 1));
      s16x4 vf[8];
#pragma unroll
      for (int u = 0; u < 8; ++u) vf[u] = __builtin_amdgcn_ds_read_tr16_b64_v4i16((LAS s16x4*)(size_t)(trb + (64 * c + 8 * u) * 256));
      SCHED_FENCE();
#pragma unroll
      for (int u = 0; u < 4; ++u)
        kv = MFMA(as_frag(af[4 * c + u]), __builtin_bit_cast(bf16x8, __builtin_shufflevector(vf[2 * u], vf[2 * u + 1], 0, 1, 2, 3, 4, 5, 6, 7)), kv);
    }
    arrive(lds + W_KV);
  }
  {
    u32x4 d0, d1;
    pack_tile(kv, d0, d1);
    ST128_DEV(kvbuf + chunk * 2048, l16, d0, 0);
    ST128_DEV(kvbuf + chunk * 2048, l16, d1, 1024);
    const unsigned zb = __builtin_bit_cast(unsigned, zown);
    const u32x2 ax = {check_word(d0, d1, zb, salt), zb};
    if (zwave) asm volatile("global_store_dwordx2 %0, %1, %2 sc1" ::"v"(lane * 8), "v"(ax), "s"(auxbuf + chunk * 512) : "memory");
    else asm volatile("global_store_dword %0, %1, %2 sc1" ::"v"(lane * 4), "v"(ax[0]), "s"(auxbuf + chunk * 512) : "memory");
  }

  if (w >= 4) __builtin_amdgcn_s_setprio(2);
  const unsigned slotQ = w < 3 ? lds + LDS_C + w * 8192 : lds + LDS_A + (w - 3) * 8192;
  u32x4 xq[4];
  {
    wait_ge(lds + W_LANDED, P_Q + 8 * (w + 1));
    u32x4 qf[8];
    unsigned qa = slotQ + xr;
    asm volatile("" : "+v"(qa));
#pragma unroll
    for (int t = 0; t < 8; ++t) qf[t] = lds_rd128(qa ^ (t << 5));
    SCHED_FENCE();
    f32x16 acc[2] = {};
    float ss[4] = {};
#pragma unroll
    for (int t = 0; t < 8; ++t)
#pragma unroll
      for (int i = 0; i < 4; ++i) ss[i] = dot2(qf[t][i], qf[t][i], ss[i]);
#pragma unroll
    for (int t = 0; t < 8; ++t) acc[0] = MFMA(as_frag(om[0][t]), as_frag(qf[t]), acc[0]);
    const float bias = add_halves((ss[0] + ss[1]) + (ss[2] + ss[3])) * c2 - 3.0f;
#pragma unroll
    for (int t = 0; t < 8; ++t) acc[1] = MFMA(as_frag(om[1][t]), as_frag(qf[t]), acc[1]);
#pragma unroll
    for (int u = 0; u < 4; ++u)
#pragma unroll
      for (int j = 0; j < 4; ++j) {
        const int e = 8 * (u & 1) + 2 * j;
        xq[u][j] = pk_bf16(__builtin_amdgcn_exp2f(acc[u >> 1][e] * c1 + bias) + 1e-6f, __builtin_amdgcn_exp2f(acc[u >> 1][e + 1] * c1 + bias) + 1e-6f);
      }
    asm volatile("" : "+v"(xq[0]), "+v"(xq[1]), "+v"(xq[2]), "+v"(xq[3]));
    SCHED_FENCE();
  }
  unsigned wb = slotQ + r * 256 + 8 * (h ^ ((r >> 3) & 1)), wx = (r & 7) << 4;
  char* dst = out + (row0 + w * 32) * 256;
  unsigned so = (lane >> 3) * 256 + (lane & 7) * 16;
  unsigned rb = slotQ + (lane >> 3) * 256 + (((lane & 7) ^ (lane >> 3)) << 4);
  unsigned kb = lds + LDS_B + l16, zb = lds + LDS_Z + h * 16;
  asm volatile("" : "+v"(wb), "+v"(wx), "+v"(so), "+v"(rb), "+v"(kb), "+v"(zb), "+s"(dst));

  __builtin_amdgcn_s_setprio(1);
  wait_ge(lds + W_KV, 8);
  wait_ge(lds + W_LANDED, P_END - GATE);
  {
    u32x4 pa[7], pb[7];
    u32x2 px[7];
    const unsigned xo = zwave ? lane * 8 : (lane >> 1) * 8;
    const bool hi = !zwave && (lane & 1);
#define REC_LOAD(i, TAIL, C)                                                                                          \
  asm volatile("global_load_dwordx4 %0, %3, %5 sc1\n\tglobal_load_dwordx4 %1, %3, %5 offset:1024 sc1\n\t"             \
               "global_load_dwordx2 %2, %4, %6 sc1" TAIL : C(pa[i]), C(pb[i]), C(px[i])                               \
               : "v"(l16), "v"(xo), "s"(kvbuf + ((chunk + 1 + (i)) & 7) * 2048), "s"(auxbuf + ((chunk + 1 + (i)) & 7) * 512) : "memory")
#define REC_BAD(i) (__builtin_amdgcn_ballot_w64(check_word(pa[i], pb[i], zwave ? px[i][1] : 0u, salt) != (hi ? px[i][1] : px[i][0])) != 0)
#pragma unroll
    for (int i = 0; i < 7; ++i) REC_LOAD(i, "", "=&v");
    if (w >= 4) __builtin_amdgcn_s_setprio(2);
    const u32x4 id0 = lds_rd128(lds + LDS_ID + l16), id1 = lds_rd128(lds + LDS_ID + 1024 + l16);
    unsigned spin = 0;
#pragma unroll
    for (int i = 0; i < 7; ++i) {
      asm volatile("s_waitcnt vmcnt(%3)" : "+v"(pa[i]), "+v"(pb[i]), "+v"(px[i]) : "n"(18 - 3 * i) : "memory");
      if (__builtin_expect(REC_BAD(i), 0)) {
        do {
          __builtin_amdgcn_s_sleep(2);
          REC_LOAD(i, "\n\ts_waitcnt vmcnt(0)", "+v");
        } while (REC_BAD(i) && ++spin < SPIN_CAP);
      }
      kv = MFMA(as_frag(id0), as_frag(pa[i]), kv);
      kv = MFMA(as_frag(id1), as_frag(pb[i]), kv);
      const unsigned zu = px[i][1];
      zown += __builtin_bit_cast(float, zu);
    }
    if (zwave && lane < 32)
      *(LAS unsigned short*)(size_t)(lds + LDS_Z + mt * 64 + (((r >> 4) * 2 + ((r >> 2) & 1)) * 8 + ((r >> 3) & 1) * 4 + (r & 3)) * 2) = (unsigned short)pk_bf16(zown, 0.f);
    u32x4 d0, d1;
    pack_tile(kv, d0, d1);
    lds_wr128(kb + (mt * 4 + dt) * 2048, d0);
    lds_wr128(kb + (mt * 4 + dt) * 2048 + 1024, d1);
    arrive(lds + W_H + (w >> 2) * 4);
  }

  {
    float inv;
#pragma unroll
    for (int half = 0; half < 2; ++half) {
      wait_ge(lds + W_H + half * 4, 4);
      u32x4 zf[4], kf2[2][4];
      if (half == 0) {
#pragma unroll
        for (int u = 0; u < 4; ++u) zf[u] = lds_rd128(zb + u * 32);
      }
#pragma unroll
      for (int d = 0; d < 2; ++d) {
        SCHED_FENCE();
#pragma unroll
        for (int u = 0; u < 4; ++u) kf2[d][u] = lds_rd128(kb + ((u >> 1) * 4 + 2 * half + d) * 2048 + (u & 1) * 1024);
      }
      SCHED_FENCE();
      f32x16 den = {}, o[2] = {};
      if (half == 0) {
#pragma unroll
        for (int u = 0; u < 4; ++u) den = MFMA(as_frag(zf[u]), as_frag(xq[u]), den);
      }
#pragma unroll
      for (int d = 0; d < 2; ++d)
#pragma unroll
        for (int u = 0; u < 4; ++u) o[d] = MFMA(as_frag(kf2[d][u]), as_frag(xq[u]), o[d]);
      if (half == 0) inv = __builtin_amdgcn_rcpf(den[0] + 1e-6f);
#pragma unroll
      for (int d = 0; d < 2; ++d)
#pragma unroll
        for (int g = 0; g < 4; ++g) {
          const u32x2 pk = {pk_bf16(o[d][4 * g] * inv, o[d][4 * g + 1] * inv), pk_bf16(o[d][4 * g + 2] * inv, o[d][4 * g + 3] * inv)};
          lds_wr64(wb + (((8 * half + 4 * d + g) << 4) ^ wx), pk);
        }
      u32x4 x[4];
#pragma unroll
      for (int i = 0; i < 4; ++i) x[i] = lds_rd128(rb + i * 2048 + half * 128);
#pragma unroll
      for (int i = 0; i < 4; ++i) {
        if (i & 1) x[i] = (u32x4){x[i][2], x[i][3], x[i][0], x[i][1]};
        ST128_DEV(dst, so + (i >> 1) * 4096, x[i], (i & 1) * 2048 + half * 128);
      }
    }
  }
  if (chunk == 0 && w == 0 && lane == 0)
    asm volatile("global_store_dword %0, %1, %2 sc1" ::"v"(0u), "v"(tag), "s"(epw) : "memory");
  asm volatile(".Lrfa_end_b:");
}

extern "C" void kernel_launch(void* const* d_in, const int* in_sizes, int n_in, void* d_out, int out_size, void* d_ws,
                              size_t ws_size, hipStream_t stream) {
  rfa_kernel<<<256, 576, 0, stream>>>((const char*)d_in[0], (const char*)d_in[1], (const char*)d_in[2], (const char*)d_in[3],
                                      (char*)d_out, (char*)d_ws);
}
```

```cpp
#include <hip/hip_runtime.h>
#include <hip/hip_bf16.h>
#include <stdint.h>

typedef __bf16 bf16x8 __attribute__((ext_vector_type(8)));
typedef __bf16 bf16x2 __attribute__((ext_vector_type(2)));
typedef short s16x4 __attribute__((ext_vector_type(4)));
typedef float f32x16 __attribute__((ext_vector_type(16)));
typedef float f32x4 __attribute__((ext_vector_type(4)));
typedef unsigned u32x4 __attribute__((ext_vector_type(4)));
typedef unsigned u32x2 __attribute__((ext_vector_type(2)));
#define LAS __attribute__((address_space(3)))
#define DI __device__ __forceinline__

#define LD_POL "nt"
constexpr int WIN = 24;
constexpr int PRO = 32;
constexpr int GATE = 16;

constexpr int S = 2048;
constexpr int LDS_A = 0;
constexpr int LDS_B = 65536;
constexpr int LDS_OM = LDS_B + 49152;
constexpr int LDS_C = 131072;
constexpr int LDS_D = 155648;
constexpr int W_LANDED = LDS_D, W_OM = LDS_D + 4, W_PHI = LDS_D + 8, W_AF = LDS_D + 12, W_KV = LDS_D + 16, W_H = LDS_D + 20;
constexpr int LDS_Z = LDS_D + 128, LDS_ID = LDS_D + 256;
constexpr int P_K = 16, P_V = 80, P_Q = 144, P_END = 208;
constexpr size_t WS_EP = 0;
constexpr size_t WS_AUX = 65536;
constexpr size_t WS_KV = WS_AUX + 1048576;
constexpr unsigned SPIN_CAP = 1u << 18;

#define MFMA(a, b, c) __builtin_amdgcn_mfma_f32_32x32x16_bf16((a), (b), (c), 0, 0, 0)
#define WAIT_VM(n) asm volatile("s_waitcnt vmcnt(%0)" ::"n"(n) : "memory")
#define SCHED_FENCE() __builtin_amdgcn_sched_barrier(0)

DI unsigned pk_bf16(float lo, float hi) { bf16x2 p; p[0] = (__bf16)lo; p[1] = (__bf16)hi; return __builtin_bit_cast(unsigned, p); }
DI bf16x8 as_frag(u32x4 v) { return __builtin_bit_cast(bf16x8, v); }
DI u32x4 lds_rd128(unsigned a) { return *(const LAS u32x4*)(size_t)a; }
DI void lds_wr128(unsigned a, u32x4 v) { *(LAS u32x4*)(size_t)a = v; }
DI void lds_wr64(unsigned a, u32x2 v) { *(LAS u32x2*)(size_t)a = v; }
DI float dot2(unsigned a, unsigned b, float c) { return __builtin_amdgcn_fdot2_f32_bf16(__builtin_bit_cast(bf16x2, a), __builtin_bit_cast(bf16x2, b), c, false); }
DI float add_halves(float x) {
  const unsigned u = __builtin_bit_cast(unsigned, x);
  const auto r = __builtin_amdgcn_permlane32_swap(u, u, false, false);
  return __builtin_bit_cast(float, (unsigned)r[0]) + __builtin_bit_cast(float, (unsigned)r[1]);
}
DI void pack_tile(const f32x16& t, u32x4& d0, u32x4& d1) {
#pragma unroll
  for (int j = 0; j < 4; ++j) { d0[j] = pk_bf16(t[2 * j], t[2 * j + 1]); d1[j] = pk_bf16(t[8 + 2 * j], t[8 + 2 * j + 1]); }
}
DI unsigned rot(unsigned x, int k) { return __builtin_amdgcn_alignbit(x, x, k); }
DI unsigned check_word(u32x4 a, u32x4 b, unsigned z, unsigned salt) {
  return ((a[0] + rot(a[1], 3) + rot(a[2], 5)) + (rot(a[3], 7) + rot(b[0], 9) + rot(b[1], 11))) + ((rot(b[2], 13) + rot(b[3], 15) + z) + salt);
}

#define DMA_QUAD(lds, g, v0, v1, v2, v3, POL)                                                   \
  asm volatile("s_mov_b32 m0, %0\n\ts_nop 0\n\t"                                                \
               "global_load_lds_dwordx4 %2, %1 offset:0 " POL "\n\t"                            \
               "global_load_lds_dwordx4 %3, %1 offset:1024 " POL "\n\t"                         \
               "global_load_lds_dwordx4 %4, %1 offset:2048 " POL "\n\t"                         \
               "global_load_lds_dwordx4 %5, %1 offset:3072 " POL                                \
               ::"s"(lds), "s"(g), "v"(v0), "v"(v1), "v"(v2), "v"(v3) : "memory")
#define ST128_DEV(g, voff, val, off)                                                            \
  asm volatile("global_store_dwordx4 %0, %1, %2 offset:%3 sc1\n\ts_nop 1" ::"v"(voff), "v"(val), "s"(g), "n"(off) : "memory")

DI void wait_ge(unsigned addr, unsigned need) {
#pragma unroll 1
  for (unsigned n = 0; n < (SPIN_CAP << 4); ++n) {
    unsigned x;
    asm volatile("ds_read_b32 %0, %1\n\ts_waitcnt lgkmcnt(0)" : "=v"(x) : "v"(addr) : "memory");
    if ((unsigned)__builtin_amdgcn_readfirstlane(x) >= need) break;
    __builtin_amdgcn_s_sleep(1);
  }
}
DI void arrive(unsigned addr) {
  unsigned long long ex;
  asm volatile("s_mov_b64 %0, exec\n\ts_mov_b64 exec, 1\n\tds_add_u32 %1, %2\n\ts_mov_b64 exec, %0" : "=&s"(ex) : "v"(addr), "v"(1u) : "memory");
}
DI void post(unsigned addr, unsigned val) { asm volatile("ds_write_b32 %0, %1" ::"v"(addr), "v"(val) : "memory"); }

__global__ __launch_bounds__(576, 3) void rfa_kernel(const char* __restrict__ q, const char* __restrict__ k,
                                                     const char* __restrict__ v, const char* __restrict__ omega,
                                                     char* __restrict__ out, char* ws) {
  __shared__ __attribute__((aligned(16))) char smem[163840];
  const unsigned lds = (unsigned)(size_t)(LAS char*)smem;
  int w = __builtin_amdgcn_readfirstlane(threadIdx.x >> 6);
  const unsigned lane = threadIdx.x & 63;
  const unsigned b = blockIdx.x, head = (b & 7) * 4 + (b >> 6), chunk = (b >> 3) & 7;
  const unsigned off0 = head * (S * 256) + chunk * 65536;
  const unsigned vs0 = (lane >> 4) * 256 + (((lane & 15) ^ (lane >> 4)) << 4), vs1 = vs0 ^ 64, vs2 = vs0 ^ 128, vs3 = vs0 ^ 192;
  asm volatile("" ::"v"(vs0), "v"(vs1), "v"(vs2), "v"(vs3), "s"(omega), "s"(k));
  if (w == 8) {
#pragma unroll 1
    for (int j = 0; j < 4; ++j) DMA_QUAD(lds + LDS_OM + j * 4096, omega + j * 4096, vs0, vs1, vs2, vs3, "");
    const char* kk = k + off0;
#pragma unroll 1
    for (int j = 0; j < (PRO - P_K) / 4; ++j) DMA_QUAD(lds + LDS_A + j * 4096, kk + j * 4096, vs0, vs1, vs2, vs3, LD_POL);
    post(lds + LDS_D + (lane & 7) * 4, 0u);
    asm volatile("s_waitcnt lgkmcnt(0)\n\ts_barrier\n.Lrfa_from:" ::: "memory");
    const unsigned vv = (lane >> 4) * 256 + (((lane & 15) ^ ((lane >> 4) << 2)) << 4);
#pragma unroll
    for (int n = PRO - 16; n > WIN - 4; n -= 4) {
      WAIT_VM(n);
      post(lds + W_LANDED, PRO - n);
    }
    unsigned done = PRO - WIN;
    auto run = [&](int quads, unsigned dst, const char* src, unsigned v0, unsigned v1, unsigned v2, unsigned v3) {
#pragma unroll 1
      for (int j = 0; j < quads; ++j) {
        DMA_QUAD(dst, src, v0, v1, v2, v3, LD_POL);
        dst += 4096; src += 4096; done += 4;
        WAIT_VM(WIN);
        post(lds + W_LANDED, done);
      }
    };
    run((P_V - PRO) / 4, lds + LDS_A + (PRO - P_K) * 1024, kk + (PRO - P_K) * 1024, vs0, vs1, vs2, vs3);
    run(12, lds + LDS_B, v + off0, vv, vv, vv, vv);
    wait_ge(lds + W_OM, 8);
    run(4, lds + LDS_B + 49152, v + off0 + 49152, vv, vv, vv, vv);
    run(6, lds + LDS_C, q + off0, vs0, vs1, vs2, vs3);
    wait_ge(lds + W_AF, 8);
    run(10, lds + LDS_A, q + off0 + 24576, vs0, vs1, vs2, vs3);
#pragma unroll
    for (int n = WIN - 4; n >= 0; n -= 4) {
      WAIT_VM(n);
      post(lds + W_LANDED, P_END - n);
    }
  }
  asm volatile("" : "+s"(w));
  if (w == 8) return;

  const unsigned r = lane & 31, h = lane >> 5, l16 = lane * 16;
  if (w == 7) {
    const unsigned pos = ((r >> 2) & 1) == h ? ((r >> 4) << 2) | (((r >> 3) & 1) << 1) | ((r >> 1) & 1) : 8u;
    const unsigned one = (r & 1) ? 0x3F800000u : 0x3F80u;
    u32x4 id0, id1;
#pragma unroll
    for (int j = 0; j < 4; ++j) { id0[j] = pos == j ? one : 0u; id1[j] = pos == 4 + j ? one : 0u; }
    lds_wr128(lds + LDS_ID + l16, id0);
    lds_wr128(lds + LDS_ID + 1024 + l16, id1);
    asm volatile("s_waitcnt lgkmcnt(0)" ::: "memory");
  }
  unsigned long long pc0;
  int from, to;
  asm volatile("s_getpc_b64 %0\n.Lrfa_pc:\n\ts_mov_b32 %1, .Lrfa_from-.Lrfa_pc\n\ts_mov_b32 %2, .Lrfa_end-.Lrfa_pc" : "=s"(pc0), "=s"(from), "=s"(to));
  unsigned warm, warm2;
  asm volatile("s_load_dword %0, %2, 0x0\n\ts_load_dword %1, %2, 0x40" : "=&s"(warm), "=&s"(warm2) : "s"(pc0 + min(from + (int)((b >> 3) * 8 + w) * 128, to - 68)) : "memory");
  asm volatile("s_barrier" ::: "memory");

  const int mt = (w >> 1) & 1, dt = (w & 1) | ((w >> 2) << 1);
  const bool zwave = dt == 0;
  char* epw = ws + WS_EP + head * 128;
  char* auxbuf = ws + WS_AUX + (size_t)(head * 8 + w) * 4096;
  char* kvbuf = ws + WS_KV + (size_t)(head * 8 + w) * 16384;
  __builtin_amdgcn_s_setprio(1);
  unsigned ep;
  asm volatile("global_load_dword %0, %1, %2 sc1" : "=v"(ep) : "v"(0u), "s"(epw) : "memory");

  const unsigned xr = r * 256 + ((h ^ (r & 15)) << 4);
  u32x4 om[2][8];
  wait_ge(lds + W_LANDED, P_K);
  asm volatile("" ::"s"(warm), "s"(warm2));
#pragma unroll
  for (int i = 0; i < 2; ++i)
#pragma unroll
    for (int t = 0; t < 8; ++t) om[i][t] = lds_rd128(((lds + LDS_OM + xr) ^ (t << 5)) + i * 8192);
  arrive(lds + W_OM);

  const float scale = 0.29730177875068026f;
  const float c1 = scale * 1.4426950408889634f;
  const float c2 = -0.5f * scale * scale * 1.4426950408889634f;

  {
    const unsigned slotA = lds + LDS_A + w * 8192;
    wait_ge(lds + W_LANDED, P_K + 8 * (w + 1));
    u32x4 kf[8];
    unsigned ka = slotA + xr;
    asm volatile("" : "+v"(ka));
#pragma unroll
    for (int t = 0; t < 8; ++t) kf[t] = lds_rd128(ka ^ (t << 5));
    SCHED_FENCE();
    f32x16 acc[2] = {};
    float ss[4] = {};
#pragma unroll
    for (int t = 0; t < 8; ++t)
#pragma unroll
      for (int i = 0; i < 4; ++i) ss[i] = dot2(kf[t][i], kf[t][i], ss[i]);
#pragma unroll
    for (int t = 0; t < 8; ++t) acc[0] = MFMA(as_frag(kf[t]), as_frag(om[0][t]), acc[0]);
    const float bias = add_halves((ss[0] + ss[1]) + (ss[2] + ss[3])) * c2 - 3.0f;
    *(LAS float*)(size_t)(slotA + 4096 + r * 4) = bias;
    f32x4 bz[4];
#pragma unroll
    for (int g = 0; g < 4; ++g) bz[g] = *(const LAS f32x4*)(size_t)(slotA + 4096 + (8 * g + 4 * h) * 4);
#pragma unroll
    for (int t = 0; t < 8; ++t) acc[1] = MFMA(as_frag(kf[t]), as_frag(om[1][t]), acc[1]);
#pragma unroll
    for (int i = 0; i < 2; ++i)
#pragma unroll
      for (int s = 0; s < 2; ++s) {
        u32x4 f;
#pragma unroll
        for (int j = 0; j < 4; ++j) {
          const int e = 8 * s + 2 * j;
          const float p0 = __builtin_amdgcn_exp2f(acc[i][e] * c1 + bz[e >> 2][e & 3]) + 1e-6f;
          const float p1 = __builtin_amdgcn_exp2f(acc[i][e + 1] * c1 + bz[(e + 1) >> 2][(e + 1) & 3]) + 1e-6f;
          f[j] = pk_bf16(p0, p1);
        }
        lds_wr128(slotA + (i * 2 + s) * 1024 + l16, f);
      }
  }
  arrive(lds + W_PHI);
  asm volatile("s_waitcnt vmcnt(0)" : "+v"(ep)::"memory");
  const unsigned tag = __builtin_amdgcn_readfirstlane(ep) + 1;
  const unsigned salt = tag + lane * 0x9E3779B1u;
  wait_ge(lds + W_PHI, 8);

  f32x16 kv = {};
  float zown = 0.f;
  {
    const unsigned ab = lds + LDS_A + mt * 2048 + l16;
    u32x4 af[16];
#pragma unroll
    for (int ks = 0; ks < 10; ++ks) af[ks] = lds_rd128(ab + (ks >> 1) * 8192 + (ks & 1) * 1024);
    arrive(lds + W_AF);
    float zs[4] = {};
    const unsigned qq = (lane & 15) >> 2, p = lane & 3, blk = (lane >> 4) & 1;
    const unsigned trb = lds + LDS_B + (4 * h + qq) * 256 + (((4 * dt + 2 * blk + (p >> 1)) ^ (qq << 2)) << 4) + 8 * (p & 1);
#pragma unroll
    for (int c = 0; c < 4; ++c) {
      if (c == 1 || c == 2) {
#pragma unroll
        for (int ks = (c == 1 ? 10 : 12); ks < (c == 1 ? 12 : 16); ++ks) af[ks] = lds_rd128(ab + (ks >> 1) * 8192 + (ks & 1) * 1024);
      }
      if (zwave && c < 3) {
#pragma unroll
        for (int ks = (c == 0 ? 0 : 4 * c + 4); ks < 4 * c + 8; ++ks)
#pragma unroll
          for (int i = 0; i < 4; ++i) zs[i] = dot2(af[ks][i], 0x3F803F80u, zs[i]);
        if (c == 2) zown = add_halves((zs[0] + zs[1]) + (zs[2] + zs[3]));
      }
      SCHED_FENCE();
      wait_ge(lds + W_LANDED, P_V + 16 * (c + 1));
      s16x4 vf[8];
#pragma unroll
      for (int u = 0; u < 8; ++u) vf[u] = __builtin_amdgcn_ds_read_tr16_b64_v4i16((LAS s16x4*)(size_t)(trb + (64 * c + 8 * u) * 256));
      SCHED_FENCE();
#pragma unroll
      for (int u = 0; u < 4; ++u)
        kv = MFMA(as_frag(af[4 * c + u]), __builtin_bit_cast(bf16x8, __builtin_shufflevector(vf[2 * u], vf[2 * u + 1], 0, 1, 2, 3, 4, 5, 6, 7)), kv);
    }
    arrive(lds + W_KV);
  }
  {
    u32x4 d0, d1;
    pack_tile(kv, d0, d1);
    ST128_DEV(kvbuf + chunk * 2048, l16, d0, 0);
    ST128_DEV(kvbuf + chunk * 2048, l16, d1, 1024);
    const unsigned zb = __builtin_bit_cast(unsigned, zown);
    const u32x2 ax = {check_word(d0, d1, zb, salt), zb};
    if (zwave) asm volatile("global_store_dwordx2 %0, %1, %2 sc1" ::"v"(lane * 8), "v"(ax), "s"(auxbuf + chunk * 512) : "memory");
    else asm volatile("global_store_dword %0, %1, %2 sc1" ::"v"(lane * 4), "v"(ax[0]), "s"(auxbuf + chunk * 512) : "memory");
  }

  if (w >= 4) __builtin_amdgcn_s_setprio(2);
  const unsigned slotQ = w < 3 ? lds + LDS_C + w * 8192 : lds + LDS_A + (w - 3) * 8192;
  u32x4 xq[4];
  {
    wait_ge(lds + W_LANDED, P_Q + 8 * (w + 1));
    u32x4 qf[8];
    unsigned qa = slotQ + xr;
    asm volatile("" : "+v"(qa));
#pragma unroll
    for (int t = 0; t < 8; ++t) qf[t] = lds_rd128(qa ^ (t << 5));
    SCHED_FENCE();
    f32x16 acc[2] = {};
    float ss[4] = {};
#pragma unroll
    for (int t = 0; t < 8; ++t)
#pragma unroll
      for (int i = 0; i < 4; ++i) ss[i] = dot2(qf[t][i], qf[t][i], ss[i]);
#pragma unroll
    for (int t = 0; t < 8; ++t) acc[0] = MFMA(as_frag(om[0][t]), as_frag(qf[t]), acc[0]);
    const float bias = add_halves((ss[0] + ss[1]) + (ss[2] + ss[3])) * c2 - 3.0f;
#pragma unroll
    for (int t = 0; t < 8; ++t) acc[1] = MFMA(as_frag(om[1][t]), as_frag(qf[t]), acc[1]);
#pragma unroll
    for (int u = 0; u < 4; ++u)
#pragma unroll
      for (int j = 0; j < 4; ++j) {
        const int e = 8 * (u & 1) + 2 * j;
        xq[u][j] = pk_bf16(__builtin_amdgcn_exp2f(acc[u >> 1][e] * c1 + bias) + 1e-6f, __builtin_amdgcn_exp2f(acc[u >> 1][e + 1] * c1 + bias) + 1e-6f);
      }
    asm volatile("" : "+v"(xq[0]), "+v"(xq[1]), "+v"(xq[2]), "+v"(xq[3]));
    SCHED_FENCE();
  }
  unsigned wb = slotQ + r * 256 + 8 * (h ^ ((r >> 3) & 1)), wx = (r & 7) << 4;
  char* dst = out + off0 + w * 8192;
  unsigned so = (lane >> 3) * 256 + (lane & 7) * 16;
  unsigned rb = slotQ + (lane >> 3) * 256 + (((lane & 7) ^ (lane >> 3)) << 4);
  unsigned kb = lds + LDS_B + l16, zb = lds + LDS_Z + h * 16;
  asm volatile("" : "+v"(wb), "+v"(wx), "+v"(so), "+v"(rb), "+v"(kb), "+v"(zb), "+s"(dst));

  __builtin_amdgcn_s_setprio(1);
  wait_ge(lds + W_KV, 8);
  wait_ge(lds + W_LANDED, P_END - GATE);
  {
    u32x4 pa[7], pb[7];
    u32x2 px[7];
    const unsigned xo = zwave ? lane * 8 : (lane >> 1) * 8;
    const bool hi = !zwave && (lane & 1);
#define REC_LOAD(i, TAIL, C)                                                                                          \
  asm volatile("global_load_dwordx4 %0, %3, %5 sc1\n\tglobal_load_dwordx4 %1, %3, %5 offset:1024 sc1\n\t"             \
               "global_load_dwordx2 %2, %4, %6 sc1" TAIL : C(pa[i]), C(pb[i]), C(px[i])                               \
               : "v"(l16), "v"(xo), "s"(kvbuf + ((chunk + 1 + (i)) & 7) * 2048), "s"(auxbuf + ((chunk + 1 + (i)) & 7) * 512) : "memory")
#define REC_BAD(i) (__builtin_amdgcn_ballot_w64(check_word(pa[i], pb[i], zwave ? px[i][1] : 0u, salt) != (hi ? px[i][1] : px[i][0])) != 0)
#pragma unroll
    for (int i = 0; i < 7; ++i) REC_LOAD(i, "", "=&v");
    if (w >= 4) __builtin_amdgcn_s_setprio(2);
    const u32x4 id0 = lds_rd128(lds + LDS_ID + l16), id1 = lds_rd128(lds + LDS_ID + 1024 + l16);
    unsigned spin = 0;
#pragma unroll
    for (int i = 0; i < 7; ++i) {
      asm volatile("s_waitcnt vmcnt(%3)" : "+v"(pa[i]), "+v"(pb[i]), "+v"(px[i]) : "n"(18 - 3 * i) : "memory");
      if (__builtin_expect(REC_BAD(i), 0)) {
        do {
          __builtin_amdgcn_s_sleep(2);
          REC_LOAD(i, "\n\ts_waitcnt vmcnt(0)", "+v");
        } while (REC_BAD(i) && ++spin < SPIN_CAP);
      }
      kv = MFMA(as_frag(id0), as_frag(pa[i]), kv);
      kv = MFMA(as_frag(id1), as_frag(pb[i]), kv);
      const unsigned zu = px[i][1];
      zown += __builtin_bit_cast(float, zu);
    }
    if (zwave && lane < 32)
      *(LAS unsigned short*)(size_t)(lds + LDS_Z + mt * 64 + (((r >> 4) * 2 + ((r >> 2) & 1)) * 8 + ((r >> 3) & 1) * 4 + (r & 3)) * 2) = (unsigned short)pk_bf16(zown, 0.f);
    u32x4 d0, d1;
    pack_tile(kv, d0, d1);
    lds_wr128(kb + (mt * 4 + dt) * 2048, d0);
    lds_wr128(kb + (mt * 4 + dt) * 2048 + 1024, d1);
    arrive(lds + W_H + (w >> 2) * 4);
  }

  {
    float inv;
#pragma unroll
    for (int half = 0; half < 2; ++half) {
      wait_ge(lds + W_H + half * 4, 4);
      u32x4 zf[4], kf2[2][4];
      if (half == 0) {
#pragma unroll
        for (int u = 0; u < 4; ++u) zf[u] = lds_rd128(zb + u * 32);
      }
#pragma unroll
      for (int d = 0; d < 2; ++d) {
        SCHED_FENCE();
#pragma unroll
        for (int u = 0; u < 4; ++u) kf2[d][u] = lds_rd128(kb + ((u >> 1) * 4 + 2 * half + d) * 2048 + (u & 1) * 1024);
      }
      SCHED_FENCE();
      f32x16 den = {}, o[2] = {};
      if (half == 0) {
#pragma unroll
        for (int u = 0; u < 4; ++u) den = MFMA(as_frag(zf[u]), as_frag(xq[u]), den);
      }
#pragma unroll
      for (int d = 0; d < 2; ++d)
#pragma unroll
        for (int u = 0; u < 4; ++u) o[d] = MFMA(as_frag(kf2[d][u]), as_frag(xq[u]), o[d]);
      if (half == 0) inv = __builtin_amdgcn_rcpf(den[0] + 1e-6f);
#pragma unroll
      for (int d = 0; d < 2; ++d)
#pragma unroll
        for (int g = 0; g < 4; ++g) {
          const u32x2 pk = {pk_bf16(o[d][4 * g] * inv, o[d][4 * g + 1] * inv), pk_bf16(o[d][4 * g + 2] * inv, o[d][4 * g + 3] * inv)};
          lds_wr64(wb + (((8 * half + 4 * d + g) << 4) ^ wx), pk);
        }
      u32x4 x[4];
#pragma unroll
      for (int i = 0; i < 4; ++i) x[i] = lds_rd128(rb + i * 2048 + half * 128);
#pragma unroll
      for (int i = 0; i < 4; ++i) {
        if (i & 1) x[i] = (u32x4){x[i][2], x[i][3], x[i][0], x[i][1]};
        ST128_DEV(dst, so + (i >> 1) * 4096, x[i], (i & 1) * 2048 + half * 128);
      }
    }
  }
  if (chunk == 0 && w == 0 && lane == 0)
    asm volatile("global_store_dword %0, %1, %2 sc1" ::"v"(0u), "v"(tag), "s"(epw) : "memory");
  asm volatile(".Lrfa_end:");
}

extern "C" void kernel_launch(void* const* d_in, const int* in_sizes, int n_in, void* d_out, int out_size, void* d_ws,
                              size_t ws_size, hipStream_t stream) {
  rfa_kernel<<<256, 576, 0, stream>>>((const char*)d_in[0], (const char*)d_in[1], (const char*)d_in[2], (const char*)d_in[3],
                                      (char*)d_out, (char*)d_ws);
}
```
